```python
import torch

def _softmax(x, axis=-1):
    return torch.softmax(x, dim=axis)
def _gelu(x):
    return torch.nn.functional.gelu(x, approximate="tanh")
def _silu(x):
    return torch.nn.functional.silu(x)
def _sigmoid(x):
    return torch.sigmoid(x)
def _layernorm(x, axis=-1, eps=1e-5):
    m = x.mean(dim=axis, keepdim=True)
    v = x.var(dim=axis, keepdim=True, unbiased=False)
    return (x - m) / torch.sqrt(v + eps)
def _scatter_add(tgt, idx, src, axis=0):
    return tgt.clone().index_add_(axis, idx, src)
def _segment_sum(src, seg, n):
    out = torch.zeros((n,) + src.shape[1:], dtype=src.dtype)
    return out.index_add_(0, seg, src)
def _pad(x, pw):
    flat = tuple(v for pair in reversed(pw) for v in pair)
    return torch.nn.functional.pad(x, flat)

kernel_name = 'gated_mix__spectral_gfnet__swiglu_ffn__prompt'

def setup_inputs():

    _w = (lambda a: (lambda t: t.to(torch.bfloat16) if t.is_floating_point() else t)(torch.as_tensor(a)))
    def _A_setup():
        B, S, D = 1, 2048, 512
        Df = D // 2 + 1
        return {
            "x": _w((lambda *s: torch.randn(*s, dtype=torch.bfloat16) * 0.05)(B, S, D)),
            "w_r": _w((lambda *s: torch.randn(*s, dtype=torch.bfloat16) * 0.8)(S, Df) * 0.02),
            "w_i": _w((lambda *s: torch.randn(*s, dtype=torch.bfloat16) * 0.8)(S, Df) * 0.02),
        }
    _A_in = _A_setup()

    def _B_setup():
        B, S, D, F = 1, 2048, 512, 1376
        return {
            "x": _w((lambda *s: torch.randn(*s, dtype=torch.bfloat16) * 0.05)(B, S, D)),
            "w_gate": _w((lambda *s: torch.randn(*s, dtype=torch.bfloat16) * 0.05)(D, F)),
            "w_up": _w((lambda *s: torch.randn(*s, dtype=torch.bfloat16) * 0.05)(D, F)),
            "w_down": _w((lambda *s: torch.randn(*s, dtype=torch.bfloat16) * 0.05)(F, D)),
        }
    _B_in = _B_setup()

    return {
        "A_x": _A_in["x"],
        "A_w_r": _A_in["w_r"],
        "A_w_i": _A_in["w_i"],
        "B_x": _B_in["x"],
        "B_w_gate": _B_in["w_gate"],
        "B_w_up": _B_in["w_up"],
        "B_w_down": _B_in["w_down"],
        "gate_w": _w((lambda *s: torch.randn(*s, dtype=torch.bfloat16) * 0.05)(512) * 16.0),
    }

def reference(A_x, A_w_r, A_w_i, B_x, B_w_gate, B_w_up, B_w_down, gate_w):
    def _A_ref(x, w_r, w_i):
        B, S, D = x.shape
        X = (lambda x, axes=(-2,-1): torch.fft.rfft2(x.float(), dim=axes))(x, axes=(-2, -1))
        W = w_r + 1j * w_i
        X = X * W
        y = (lambda x, s, axes=(-2,-1): torch.fft.irfft2(x, s=s, dim=axes).to(torch.bfloat16))(X, (S, D), axes=(-2, -1))
        return y

    def _B_ref(x, w_gate, w_up, w_down):
        g = _silu(x @ w_gate)
        u = x @ w_up
        return (g * u) @ w_down

    x = A_x
    g = _sigmoid((x * gate_w).sum(-1, keepdims=True))
    a = _A_ref(x, A_w_r, A_w_i)
    b = _B_ref(x, B_w_gate, B_w_up, B_w_down)
    return g * a + (1 - g) * b
```

```cpp
#if defined(__HIP_DEVICE_COMPILE__) && __HIP_DEVICE_COMPILE__
#pragma clang attribute push(__attribute__((target("no-packed-fp32-ops"))), apply_to = function)
#define POP_TARGET_ATTR 1
#endif
#include <hip/hip_runtime.h>
#include <hip/hip_bf16.h>
#include <stdint.h>
#define TID_X ((int)__builtin_amdgcn_workitem_id_x())
#define BID_X ((int)__builtin_amdgcn_workgroup_id_x())

typedef short bf16x8 __attribute__((ext_vector_type(8)));
typedef short s16x4 __attribute__((ext_vector_type(4)));
typedef float f32x2 __attribute__((ext_vector_type(2)));
typedef float f32x4 __attribute__((ext_vector_type(4)));
typedef float f32x16 __attribute__((ext_vector_type(16)));
typedef unsigned u32x4 __attribute__((ext_vector_type(4)));
typedef unsigned u32x2 __attribute__((ext_vector_type(2)));
typedef __bf16 bf16x2_t __attribute__((ext_vector_type(2)));
typedef __attribute__((address_space(3))) s16x4* lds_s16x4_ptr;
typedef __attribute__((address_space(3))) void* lds_void_ptr;
typedef const __attribute__((address_space(1))) void* glb_cvoid_ptr;

#define DI __device__ __forceinline__
#define SB() __builtin_amdgcn_sched_barrier(0)

constexpr int S_ = 2048, D_ = 512, F_ = 1376, FP_ = 1408, DF_ = 257;
constexpr int XS_ = 2080;

constexpr size_t WS_XT = 0;
constexpr size_t WS_H = 5u << 20;
constexpr size_t WS_B = 11u << 20;
constexpr size_t WS_WT = 13u << 20;
constexpr size_t WS_G = 16u << 20;
constexpr size_t WS_YT = 17u << 20;

#define TRACE_BEGIN
#define TRACE_END(ws, k)

struct cf { float x, y; };
DI cf mk(float x, float y) { cf r; r.x = x; r.y = y; return r; }

DI cf tw_get(int j) {
  const float r = (float)(j & 2047) * (1.f / 2048.f);
  return mk(__builtin_amdgcn_cosf(r), -__builtin_amdgcn_sinf(r));
}
DI unsigned pk_bf16(float a, float b) {
  f32x2 v = {a, b};
  return __builtin_bit_cast(unsigned, __builtin_convertvector(v, bf16x2_t));
}
DI float lo_f(unsigned w) { return __builtin_bit_cast(float, w << 16); }
DI float hi_f(unsigned w) { return __builtin_bit_cast(float, w & 0xffff0000u); }
DI float rbf(float f) { return lo_f(pk_bf16(f, f)); }
DI float sigm(float z) { return __builtin_amdgcn_rcpf(1.f + __builtin_amdgcn_exp2f(-1.4426950408889634f * z)); }

DI cf cadd(cf a, cf b) { return mk(a.x + b.x, a.y + b.y); }
DI cf csub(cf a, cf b) { return mk(a.x - b.x, a.y - b.y); }
DI cf cmul(cf a, cf b) { return mk(a.x * b.x - a.y * b.y, a.x * b.y + a.y * b.x); }
DI cf cmulc(cf a, cf b) { return mk(a.x * b.x + a.y * b.y, a.y * b.x - a.x * b.y); }
template <int SGN> DI cf mul_i(cf a) { return SGN < 0 ? mk(a.y, -a.x) : mk(-a.y, a.x); }

template <int SGN> DI void dft4(cf& b0, cf& b1, cf& b2, cf& b3) {
  cf c0 = cadd(b0, b2), c1 = csub(b0, b2), c2 = cadd(b1, b3), c3 = mul_i<SGN>(csub(b1, b3));
  b0 = cadd(c0, c2); b2 = csub(c0, c2); b1 = cadd(c1, c3); b3 = csub(c1, c3);
}
template <int SGN> DI void dft8(cf* v) {
  const float r = 0.70710678118654752440f;
  cf a0 = cadd(v[0], v[4]), a4 = csub(v[0], v[4]);
  cf a1 = cadd(v[1], v[5]), a5 = csub(v[1], v[5]);
  cf a2 = cadd(v[2], v[6]), a6 = csub(v[2], v[6]);
  cf a3 = cadd(v[3], v[7]), a7 = csub(v[3], v[7]);
  if (SGN < 0) {
    a5 = mk((a5.x + a5.y) * r, (a5.y - a5.x) * r);
    a7 = mk((a7.y - a7.x) * r, -(a7.x + a7.y) * r);
  } else {
    a5 = mk((a5.x - a5.y) * r, (a5.y + a5.x) * r);
    a7 = mk(-(a7.x + a7.y) * r, (a7.x - a7.y) * r);
  }
  a6 = mul_i<SGN>(a6);
  dft4<SGN>(a0, a1, a2, a3);
  dft4<SGN>(a4, a5, a6, a7);
  v[0] = a0; v[2] = a1; v[4] = a2; v[6] = a3;
  v[1] = a4; v[3] = a5; v[5] = a6; v[7] = a7;
}

DI void wave_sync() {
  __builtin_amdgcn_fence(__ATOMIC_RELEASE, "wavefront");
  __builtin_amdgcn_wave_barrier();
  __builtin_amdgcn_fence(__ATOMIC_ACQUIRE, "wavefront");
}

constexpr int RGN = 292;

DI void fft256_fwd(cf* v, cf* sc, int j, const cf* tw1, const cf* tw2) {
  dft8<-1>(v);
#pragma unroll
  for (int k = 1; k < 8; ++k) v[k] = cmul(v[k], tw1[k - 1]);
#pragma unroll
  for (int k = 0; k < 8; ++k) sc[k * 36 + j] = v[k];
  wave_sync();
  const int k1 = j >> 2, a = j & 3;
#pragma unroll
  for (int m = 0; m < 8; ++m) v[m] = sc[k1 * 36 + 4 * m + a];
  wave_sync();
  dft8<-1>(v);
#pragma unroll
  for (int k = 1; k < 8; ++k) v[k] = cmul(v[k], tw2[k - 1]);
#pragma unroll
  for (int k = 0; k < 8; ++k) sc[k * 33 + j] = v[k];
  wave_sync();
#pragma unroll
  for (int e = 0; e < 2; ++e)
#pragma unroll
    for (int m = 0; m < 4; ++m) v[e * 4 + m] = sc[(a + 4 * e) * 33 + k1 * 4 + m];
  wave_sync();
  dft4<-1>(v[0], v[1], v[2], v[3]);
  dft4<-1>(v[4], v[5], v[6], v[7]);
}
DI void fft256_inv(cf* v, cf* sc, int j, const cf* tw1, const cf* tw2) {
  const int k1 = j >> 2, a = j & 3;
  dft4<1>(v[0], v[1], v[2], v[3]);
  dft4<1>(v[4], v[5], v[6], v[7]);
#pragma unroll
  for (int e = 0; e < 2; ++e)
#pragma unroll
    for (int m = 0; m < 4; ++m) sc[(a + 4 * e) * 33 + k1 * 4 + m] = v[e * 4 + m];
  wave_sync();
#pragma unroll
  for (int k = 0; k < 8; ++k) v[k] = sc[k * 33 + j];
  wave_sync();
#pragma unroll
  for (int k = 1; k < 8; ++k) v[k] = cmulc(v[k], tw2[k - 1]);
  dft8<1>(v);
#pragma unroll
  for (int m = 0; m < 8; ++m) sc[k1 * 36 + 4 * m + a] = v[m];
  wave_sync();
#pragma unroll
  for (int k = 0; k < 8; ++k) v[k] = sc[k * 36 + j];
  wave_sync();
#pragma unroll
  for (int k = 1; k < 8; ++k) v[k] = cmulc(v[k], tw1[k - 1]);
  dft8<1>(v);
}
DI void load_tw256(int j, cf* tw1, cf* tw2) {
#pragma unroll
  for (int k = 1; k < 8; ++k) tw1[k - 1] = tw_get(8 * j * k);
#pragma unroll
  for (int k = 1; k < 8; ++k) tw2[k - 1] = tw_get(64 * (j & 3) * k);
}

#define SM_H 0
#define SM_X 0
#define SM_Y 0
#define SM_B 0
template <int MODE> DI void st16(void* p, u32x4 v) {
  if (MODE == 0) *reinterpret_cast<u32x4*>(p) = v;
  else asm volatile("global_store_dwordx4 %0, %1, off sc1\n\ts_nop 1" ::"v"(p), "v"(v) : "memory");
}
template <int MODE> DI void st8(void* p, u32x2 v) {
  if (MODE == 0) *reinterpret_cast<u32x2*>(p) = v;
  else asm volatile("global_store_dwordx2 %0, %1, off sc1" ::"v"(p), "v"(v) : "memory");
}
DI u32x4 f4u(f32x4 v) { return __builtin_bit_cast(u32x4, v); }

DI void glds16(const void* g, char* lds) {
  __builtin_amdgcn_global_load_lds((glb_cvoid_ptr)g, (lds_void_ptr)lds, 16, 0, 0);
}

constexpr int G1_TILES = 240;
constexpr int AUX_WG = 256;
constexpr int K1_GRID = G1_TILES + AUX_WG;
constexpr int K1_LDS = 81920;

DI void gemm1_role(char* smem, int b, const unsigned short* __restrict__ x, const unsigned short* __restrict__ wg,
                   const unsigned short* __restrict__ wu, unsigned short* __restrict__ H) {
  const int tid = TID_X, lane = tid & 63, wid = tid >> 6;
  const int xcd = b & 7, li = b >> 3;
  const int ft = li >> 1, mt = xcd * 2 + (li & 1);
  const int m0 = mt * 128, f0 = ft * 96;
  char* const As = smem;
  char* const Bs = smem + 32768;

  const unsigned short* asrc[2];
#pragma unroll
  for (int i = 0; i < 2; ++i) {
    const int row = (wid * 2 + i) * 16 + (lane >> 2), p = lane & 3;
    asrc[i] = x + (size_t)(m0 + row) * D_ + (p ^ ((4 - ((row >> 2) & 3)) & 3)) * 8;
  }
  const unsigned short* bsrc[3];
#pragma unroll
  for (int i = 0; i < 3; ++i) {
    const int n16 = 4 * i + (lane >> 4);
    const int rp = (lane >> 1) & 7, hf = lane & 1;
    const int k = wid * 8 + (rp ^ (4 * (wid & 1)));
    int col = f0 + (n16 % 6) * 16 + hf * 8;
    col = col > F_ - 8 ? F_ - 8 : col;
    bsrc[i] = (n16 < 6 ? wg : wu) + (size_t)k * F_ + col;
  }
  auto glA = [&](int tt, int i) { glds16(asrc[i] + tt * 32, As + (tt & 3) * 8192 + (wid * 2 + i) * 1024); };
  auto glB = [&](int tt, int i) { glds16(bsrc[i] + (size_t)tt * 32 * F_, Bs + (tt & 3) * 12288 + (wid * 3 + i) * 1024); };

  const int wm = wid >> 1, wn = wid & 1;
  const int g = lane >> 4, q = (lane & 15) >> 2, p = lane & 3;
  int aoff[4];
#pragma unroll
  for (int i = 0; i < 4; ++i) {
    const int row = wm * 64 + i * 16 + (lane & 15);
    aoff[i] = row * 64 + ((g ^ ((4 - ((row >> 2) & 3)) & 3)) << 4);
  }
  const int bo0 = (g * 12 + wn * 3) * 256 + ((q ^ (4 * (g & 1))) * 32) + p * 8;
  const int bo1 = (g * 12 + wn * 3) * 256 + (((4 + q) ^ (4 * (g & 1))) * 32) + p * 8;

  f32x4 acc[4][6];
#pragma unroll
  for (int i = 0; i < 4; ++i)
#pragma unroll
    for (int j = 0; j < 6; ++j) acc[i][j] = (f32x4){0.f, 0.f, 0.f, 0.f};

  bf16x8 af[2][4];
  s16x4 bl[2][6], bh[2][6];
  auto dsr = [&](int t, int set, int n) {
    const char* ab = As + (t & 3) * 8192;
    const char* bb = Bs + (t & 3) * 12288;
    if (n < 12) {
      const int j = n >> 1, o = (j < 3 ? j : j + 3) * 256;
      if (n & 1) bh[set][j] = __builtin_amdgcn_ds_read_tr16_b64_v4i16((lds_s16x4_ptr)(bb + bo1 + o));
      else bl[set][j] = __builtin_amdgcn_ds_read_tr16_b64_v4i16((lds_s16x4_ptr)(bb + bo0 + o));
    } else {
      af[set][n - 12] = *reinterpret_cast<const bf16x8*>(ab + aoff[n - 12]);
    }
  };
  auto gl = [&](int t, int gi) {
    if (t & 1) {
      if (gi < 4) { glA(t + 3 + (gi & 1), gi >> 1); return; }
      gi -= 4;
    }
    if (gi < 3) glB(t + 4, gi);
  };
  auto mm = [&](int set, int i) {
    const int mi = i / 6, j = i % 6;
    acc[mi][j] = __builtin_amdgcn_mfma_f32_16x16x32_bf16(__builtin_shufflevector(bl[set][j], bh[set][j], 0, 1, 2, 3, 4, 5, 6, 7),
                                                         af[set][mi], acc[mi][j], 0, 0, 0);
  };

  glA(0, 0); glA(1, 0); glA(0, 1); glA(1, 1);
  glB(0, 0); glB(0, 1); glB(0, 2); glB(1, 0); glB(1, 1); glB(1, 2);
  glA(2, 0); glA(3, 0); glA(2, 1); glA(3, 1);
  glB(2, 0); glB(2, 1); glB(2, 2); glB(3, 0); glB(3, 1); glB(3, 2);
  asm volatile("s_waitcnt vmcnt(13)\n\ts_barrier" ::: "memory");
#pragma unroll
  for (int n = 0; n < 16; ++n) dsr(0, 0, n);
  SB();
#pragma unroll
  for (int t = 0; t < 16; ++t) {
    if (t < 15) {
      if (t == 14) asm volatile("s_waitcnt vmcnt(0) lgkmcnt(0)\n\ts_barrier" ::: "memory");
      else if (t == 13) asm volatile("s_waitcnt vmcnt(3) lgkmcnt(0)\n\ts_barrier" ::: "memory");
      else if (t & 1) asm volatile("s_waitcnt vmcnt(6) lgkmcnt(0)\n\ts_barrier" ::: "memory");
      else asm volatile("s_waitcnt vmcnt(10) lgkmcnt(0)\n\ts_barrier" ::: "memory");
    }
    SB();
#pragma unroll
    for (int i = 0; i < 24; ++i) {
      mm(t & 1, i);
      SB();
      if (t < 15 && i < 16) { dsr(t + 1, (t + 1) & 1, i); SB(); }
      if (t + 4 < 16 && (i % 3) == 2) {
        const int gi = i / 3;
        if (gi < ((t & 1) ? 7 : 3)) { gl(t, gi); SB(); }
      }
    }
  }
  asm volatile("s_waitcnt vmcnt(0) lgkmcnt(0)\n\ts_barrier" ::: "memory");

  constexpr int HS = 56;
  unsigned short* tl = reinterpret_cast<unsigned short*>(smem) + wid * (64 * HS);
#pragma unroll
  for (int mi = 0; mi < 4; ++mi)
#pragma unroll
    for (int j = 0; j < 3; ++j) {
      u32x2 o;
#pragma unroll
      for (int e = 0; e < 2; ++e) {
        const unsigned gp = pk_bf16(acc[mi][j][2 * e], acc[mi][j][2 * e + 1]);
        const unsigned up = pk_bf16(acc[mi][j + 3][2 * e], acc[mi][j + 3][2 * e + 1]);
        const float g0 = lo_f(gp), g1 = hi_f(gp);
        const unsigned sp = pk_bf16(g0 * sigm(g0), g1 * sigm(g1));
        o[e] = pk_bf16(lo_f(sp) * lo_f(up), hi_f(sp) * hi_f(up));
      }
      *reinterpret_cast<u32x2*>(tl + (mi * 16 + (lane & 15)) * HS + j * 16 + g * 4) = o;
    }
  wave_sync();
#pragma unroll
  for (int i = 0; i < 6; ++i) {
    const int c = lane + 64 * i, row = c / 6, ch = c % 6;
    const int f = f0 + wn * 48 + ch * 8;
    if (f < FP_) {
      u32x4 v = *reinterpret_cast<const u32x4*>(tl + row * HS + ch * 8);
      if (f >= F_) v = (u32x4){0u, 0u, 0u, 0u};
      st16<SM_H>(H + (size_t)(m0 + wm * 64 + row) * FP_ + f, v);
    }
  }
}

DI void aux1_role(char* smem, int b, const unsigned short* __restrict__ x, const unsigned short* __restrict__ gw,
                  const unsigned short* __restrict__ wr, const unsigned short* __restrict__ wi, cf* __restrict__ XT,
                  float* __restrict__ G, unsigned* __restrict__ WT) {
  const int tid = TID_X, j = tid & 31, hw = tid >> 5;
  const int s0 = b * 8, s = s0 + hw;
  cf* sc = reinterpret_cast<cf*>(smem) + hw * RGN;
  const unsigned* xr = reinterpret_cast<const unsigned*>(x + (size_t)s * D_);
  const unsigned* gr = reinterpret_cast<const unsigned*>(gw);
  unsigned xv[8], gv[8];
#pragma unroll
  for (int r = 0; r < 8; ++r) xv[r] = xr[j + 32 * r];
#pragma unroll
  for (int r = 0; r < 8; ++r) gv[r] = gr[j + 32 * r];
  const u32x4* sr4 = reinterpret_cast<const u32x4*>(wr + (size_t)s0 * DF_);
  const u32x4* si4 = reinterpret_cast<const u32x4*>(wi + (size_t)s0 * DF_);
  const u32x4 w0 = sr4[tid], w1 = si4[tid];
  u32x4 w2 = w0;
  if (tid < 2) w2 = (tid == 0 ? sr4 : si4)[256];
  cf tw1[7], tw2[7];
  load_tw256(j, tw1, tw2);
  u32x4* lw = reinterpret_cast<u32x4*>(smem + 20480);
  lw[tid] = w0; lw[258 + tid] = w1;
  if (tid < 2) lw[tid * 258 + 256] = w2;

  cf v[8];
  float gs = 0.f;
#pragma unroll
  for (int r = 0; r < 8; ++r) {
    v[r] = mk(lo_f(xv[r]), hi_f(xv[r]));
    const unsigned pp = pk_bf16(v[r].x * lo_f(gv[r]), v[r].y * hi_f(gv[r]));
    gs += lo_f(pp);
    gs += hi_f(pp);
  }
#pragma unroll
  for (int o = 16; o > 0; o >>= 1) gs += __shfl_xor(gs, o);
  if (j == 0) G[s] = rbf(sigm(rbf(gs)));
  fft256_fwd(v, sc, j, tw1, tw2);
  {
    const int k1 = j >> 2, a = j & 3;
#pragma unroll
    for (int e = 0; e < 2; ++e)
#pragma unroll
      for (int l = 0; l < 4; ++l) sc[k1 + 8 * (a + 4 * e) + 64 * l] = v[e * 4 + l];
  }
  __syncthreads();
  {
    const int sp = (tid & 3) * 2, kk = tid >> 2;
    const cf* z0 = reinterpret_cast<const cf*>(smem) + sp * RGN;
    const cf* z1 = z0 + RGN;
#pragma unroll
    for (int i = 0; i < 2; ++i) {
      const int k = kk + 64 * i;
      if (k == 0) {
        const cf a0 = z0[0], ah = z0[128], b0 = z1[0], bh = z1[128];
        st16<SM_X>(XT + s0 + sp, f4u((f32x4){a0.x + a0.y, a0.x - a0.y, b0.x + b0.y, b0.x - b0.y}));
        st16<SM_X>(XT + (size_t)128 * XS_ + s0 + sp, f4u((f32x4){ah.x, -ah.y, bh.x, -bh.y}));
      } else {
        const cf w = tw_get(4 * k);
        f32x4 o0, o1;
#pragma unroll
        for (int rr = 0; rr < 2; ++rr) {
          const cf za = (rr ? z1 : z0)[k], zb = (rr ? z1 : z0)[256 - k];
          const cf E = mk(0.5f * (za.x + zb.x), 0.5f * (za.y - zb.y));
          const cf O = mk(0.5f * (za.y + zb.y), 0.5f * (zb.x - za.x));
          const cf wo = cmul(O, w);
          o0[2 * rr] = E.x + wo.x; o0[2 * rr + 1] = E.y + wo.y;
          o1[2 * rr] = E.x - wo.x; o1[2 * rr + 1] = wo.y - E.y;
        }
        st16<SM_X>(XT + (size_t)k * XS_ + s0 + sp, f4u(o0));
        st16<SM_X>(XT + (size_t)(256 - k) * XS_ + s0 + sp, f4u(o1));
      }
    }
  }
  {
    const unsigned short* fr = reinterpret_cast<const unsigned short*>(lw);
    const unsigned short* fi = reinterpret_cast<const unsigned short*>(lw + 258);
    const int r4 = (tid & 1) * 4;
#pragma unroll
    for (int it = 0; it < 3; ++it) {
      const int c = (tid >> 1) + 128 * it;
      if (c < DF_) {
        u32x4 o;
#pragma unroll
        for (int e = 0; e < 4; ++e) o[e] = (unsigned)fr[(r4 + e) * DF_ + c] | ((unsigned)fi[(r4 + e) * DF_ + c] << 16);
        st16<SM_X>(WT + (size_t)c * XS_ + s0 + r4, o);
      }
    }
  }
}

__global__ __launch_bounds__(256, 2) void k1_kernel(const unsigned short* __restrict__ x, const unsigned short* __restrict__ wr,
                                                    const unsigned short* __restrict__ wi, const unsigned short* __restrict__ wg,
                                                    const unsigned short* __restrict__ wu, const unsigned short* __restrict__ gw,
                                                    char* __restrict__ ws) {
  extern __shared__ __attribute__((aligned(16))) char smem[];
  const int b = BID_X;
  TRACE_BEGIN
  if (b < G1_TILES) {
    gemm1_role(smem, b, x, wg, wu, reinterpret_cast<unsigned short*>(ws + WS_H));
  } else {
    aux1_role(smem, ((b - G1_TILES) & 7) * 32 + ((b - G1_TILES) >> 3), x, gw, wr, wi, reinterpret_cast<cf*>(ws + WS_XT), reinterpret_cast<float*>(ws + WS_G),
              reinterpret_cast<unsigned*>(ws + WS_WT));
  }
  TRACE_END(ws, 0)
}

constexpr int G2_TILES = 256;
constexpr int K2_GRID = G2_TILES + 256;
constexpr int K2_LDS = 81920;

DI void gemm2_role(char* smem, int b, const unsigned short* __restrict__ H, const unsigned short* __restrict__ wd,
                   unsigned short* __restrict__ Bo) {
  const int tid = TID_X, lane = tid & 63, wid = tid >> 6;
  const int xcd = b & 7, li = b >> 3;
  const int mt = xcd * 4 + (li >> 3), nt = li & 7;
  const int m0 = mt * 64, n0 = nt * 64;
  const unsigned short* asrc[2];
  int bk[2];
#pragma unroll
  for (int i = 0; i < 2; ++i) {
    const int row = (wid * 2 + i) * 8 + (lane >> 3), c = (lane & 7) ^ ((row >> 1) & 7);
    asrc[i] = H + (size_t)(m0 + row) * FP_ + c * 8;
    bk[i] = (wid * 2 + i) * 8 + (((lane >> 1) & 7) ^ (4 * ((lane >> 4) & 1)));
  }
  const unsigned short* bsrc = wd + n0 + (lane >> 4) * 16 + (lane & 1) * 8;
  auto gl = [&](int t, int i) {
    char* buf = smem + (t % 5) * 16384;
    if (i < 2) glds16(asrc[i] + t * 64, buf + (wid * 2 + i) * 1024);
    else {
      int k = t * 64 + bk[i - 2];
      if (t * 64 + 63 >= F_) k = k < F_ ? k : F_ - 1;
      glds16(bsrc + (size_t)k * D_, buf + 8192 + (wid * 2 + i - 2) * 1024);
    }
  };
  const int wm = wid >> 1, wn = wid & 1;
  const int h = lane >> 5, gi = lane >> 4, q = (lane & 15) >> 2, p = lane & 3;
  const int arow = wm * 32 + (lane & 31);
  int aoff[4];
#pragma unroll
  for (int ks = 0; ks < 4; ++ks) aoff[ks] = arow * 128 + (((2 * ks + h) ^ ((arow >> 1) & 7)) << 4);
  const int bbase = 8192 + (h * 4 + wn * 2 + (gi & 1)) * 256 + p * 8;
  const int bo0 = bbase + ((q ^ (4 * (gi & 1))) * 32), bo1 = bbase + (((4 + q) ^ (4 * (gi & 1))) * 32);
  f32x16 acc;
#pragma unroll
  for (int r = 0; r < 16; ++r) acc[r] = 0.f;
  bf16x8 a[2][4];
  s16x4 bl[2][4], bh[2][4];
  auto dsr = [&](int t, int set, int ks) {
    const char* buf = smem + (t % 5) * 16384;
    a[set][ks] = *reinterpret_cast<const bf16x8*>(buf + aoff[ks]);
    bl[set][ks] = __builtin_amdgcn_ds_read_tr16_b64_v4i16((lds_s16x4_ptr)(buf + bo0 + ks * 2048));
    bh[set][ks] = __builtin_amdgcn_ds_read_tr16_b64_v4i16((lds_s16x4_ptr)(buf + bo1 + ks * 2048));
  };
  constexpr int NT = 22;
#pragma unroll
  for (int t = 0; t < 5; ++t)
#pragma unroll
    for (int i = 0; i < 4; ++i) gl(t, i);
  asm volatile("s_waitcnt vmcnt(16)\n\ts_barrier" ::: "memory");
#pragma unroll
  for (int i = 0; i < 4; ++i) dsr(0, 0, i);
  SB();
#pragma unroll
  for (int t = 0; t < NT; ++t) {
    if (t < NT - 1) {
      const int later = (t + 4 < NT ? t + 4 : NT - 1) - (t + 1);
      if (later == 3) asm volatile("s_waitcnt vmcnt(12) lgkmcnt(0)\n\ts_barrier" ::: "memory");
      else if (later == 2) asm volatile("s_waitcnt vmcnt(8) lgkmcnt(0)\n\ts_barrier" ::: "memory");
      else if (later == 1) asm volatile("s_waitcnt vmcnt(4) lgkmcnt(0)\n\ts_barrier" ::: "memory");
      else asm volatile("s_waitcnt vmcnt(0) lgkmcnt(0)\n\ts_barrier" ::: "memory");
    }
    SB();
#pragma unroll
    for (int i = 0; i < 4; ++i) {
      acc = __builtin_amdgcn_mfma_f32_32x32x16_bf16(__builtin_shufflevector(bl[t & 1][i], bh[t & 1][i], 0, 1, 2, 3, 4, 5, 6, 7),
                                                    a[t & 1][i], acc, 0, 0, 0);
      SB();
      if (t < NT - 1) { dsr(t + 1, (t + 1) & 1, i); SB(); }
      if (t + 5 < NT) { gl(t + 5, i); SB(); }
    }
  }
  unsigned short* drow = Bo + (size_t)(m0 + arow) * D_ + n0 + wn * 32 + h * 8;
#pragma unroll
  for (int rp = 0; rp < 2; ++rp) {
    unsigned e0 = pk_bf16(acc[8 * rp], acc[8 * rp + 1]), e1 = pk_bf16(acc[8 * rp + 2], acc[8 * rp + 3]);
    unsigned o0 = pk_bf16(acc[8 * rp + 4], acc[8 * rp + 5]), o1 = pk_bf16(acc[8 * rp + 6], acc[8 * rp + 7]);
    auto r0 = __builtin_amdgcn_permlane32_swap(e0, o0, false, false);
    auto r1 = __builtin_amdgcn_permlane32_swap(e1, o1, false, false);
    st16<SM_B>(drow + rp * 16, (u32x4){r0[0], r1[0], r0[1], r1[1]});
  }
}

DI int col_u(int t, int slot) {
  const int j = t & 31;
  return (t >> 5) + 8 * ((j >> 2) + 8 * ((j & 3) + 4 * (slot >> 2)) + 64 * (slot & 3));
}

DI void fft2_role(char* smem, int col, const cf* __restrict__ XT, const unsigned* __restrict__ WT, cf* __restrict__ YT) {
  const int t = TID_X, j = t & 31, hw = t >> 5;
  cf* all = reinterpret_cast<cf*>(smem);
  cf* sc = all + hw * RGN;
  const cf* xc = XT + (size_t)col * XS_;
  cf v[8];
#pragma unroll
  for (int r = 0; r < 8; ++r) v[r] = xc[t + 256 * r];
  unsigned w[8], wm[8], wn[8], wnm[8];
#pragma unroll
  for (int r = 0; r < 8; ++r) w[r] = WT[(size_t)col * XS_ + col_u(t, r)];
  if (col == 0) {
#pragma unroll
    for (int r = 0; r < 8; ++r) {
      const int u = col_u(t, r), um = (S_ - u) & (S_ - 1);
      wm[r] = WT[um]; wn[r] = WT[(size_t)256 * XS_ + u]; wnm[r] = WT[(size_t)256 * XS_ + um];
    }
  }
  cf tw0[7], tw1[7], tw2[7];
#pragma unroll
  for (int k = 1; k < 8; ++k) tw0[k - 1] = tw_get(t * k);
  load_tw256(j, tw1, tw2);

  dft8<-1>(v);
#pragma unroll
  for (int k = 1; k < 8; ++k) v[k] = cmul(v[k], tw0[k - 1]);
#pragma unroll
  for (int k = 0; k < 8; ++k) all[k * RGN + t] = v[k];
  __syncthreads();
#pragma unroll
  for (int r = 0; r < 8; ++r) v[r] = sc[j + 32 * r];
  wave_sync();
  fft256_fwd(v, sc, j, tw1, tw2);

  if (col != 0) {
#pragma unroll
    for (int r = 0; r < 8; ++r) v[r] = cmul(v[r], mk(lo_f(w[r]), hi_f(w[r])));
  } else {
    cf* nat = all + 8 * RGN;
#pragma unroll
    for (int r = 0; r < 8; ++r) { const int u = col_u(t, r); nat[(u & 7) * 260 + (u >> 3)] = v[r]; }
    __syncthreads();
#pragma unroll
    for (int r = 0; r < 8; ++r) {
      const int um = (S_ - col_u(t, r)) & (S_ - 1);
      const cf P = v[r], M = nat[(um & 7) * 260 + (um >> 3)];
      const cf X0 = mk(0.5f * (P.x + M.x), 0.5f * (P.y - M.y));
      const cf XN = mk(0.5f * (P.y + M.y), 0.5f * (M.x - P.x));
      const cf W0 = mk(0.5f * (lo_f(w[r]) + lo_f(wm[r])), 0.5f * (hi_f(w[r]) - hi_f(wm[r])));
      const cf WN = mk(0.5f * (lo_f(wn[r]) + lo_f(wnm[r])), 0.5f * (hi_f(wn[r]) - hi_f(wnm[r])));
      const cf Z0 = cmul(X0, W0), ZN = cmul(XN, WN);
      v[r] = mk(Z0.x - ZN.y, Z0.y + ZN.x);
    }
  }

  fft256_inv(v, sc, j, tw1, tw2);
#pragma unroll
  for (int r = 0; r < 8; ++r) sc[j + 32 * r] = v[r];
  __syncthreads();
#pragma unroll
  for (int k = 0; k < 8; ++k) v[k] = all[k * RGN + t];
#pragma unroll
  for (int k = 1; k < 8; ++k) v[k] = cmulc(v[k], tw0[k - 1]);
  dft8<1>(v);
#pragma unroll
  for (int r = 0; r < 8; ++r)
    st8<SM_Y>(YT + ((size_t)((t >> 3) + 32 * r) * 256 + col) * 8 + (t & 7), (u32x2){__builtin_bit_cast(unsigned, v[r].x), __builtin_bit_cast(unsigned, v[r].y)});
}

__global__ __launch_bounds__(256, 2) void k2_kernel(const unsigned short* __restrict__ wd, char* __restrict__ ws) {
  extern __shared__ __attribute__((aligned(16))) char smem[];
  const int b = BID_X;
  TRACE_BEGIN
  if (b >= 256) {
    gemm2_role(smem, b - 256, reinterpret_cast<const unsigned short*>(ws + WS_H), wd, reinterpret_cast<unsigned short*>(ws + WS_B));
  } else {
    fft2_role(smem, (b & 7) * 32 + (b >> 3), reinterpret_cast<const cf*>(ws + WS_XT), reinterpret_cast<const unsigned*>(ws + WS_WT),
              reinterpret_cast<cf*>(ws + WS_YT));
  }
  TRACE_END(ws, 1)
}

constexpr int R3 = 322;
__global__ __launch_bounds__(512) void k3_kernel(const char* __restrict__ ws, unsigned short* __restrict__ out) {
  __shared__ __attribute__((aligned(16))) cf smem[8 * R3];
  TRACE_BEGIN
  const int tid = TID_X, lane = tid & 63, wv = tid >> 6;
  const int blk = (BID_X & 7) * 32 + (BID_X >> 3);
  const int s0 = blk * 8, s = s0 + wv;
  const cf* YT = reinterpret_cast<const cf*>(ws + WS_YT) + (size_t)blk * 2048;
  const unsigned* Bo = reinterpret_cast<const unsigned*>(ws + WS_B) + (size_t)s * (D_ / 2);
  const float* G = reinterpret_cast<const float*>(ws + WS_G);
  const int sr = tid & 7, kk = tid >> 3;
  cf ya[2], yb[2], twk[2];
#pragma unroll
  for (int i = 0; i < 2; ++i) {
    const int k = kk + 64 * i;
    ya[i] = YT[k * 8 + sr];
    yb[i] = YT[(k == 0 ? 128 : 256 - k) * 8 + sr];
  }
  unsigned bv[4];
#pragma unroll
  for (int r = 0; r < 4; ++r) bv[r] = Bo[lane + 64 * r];
  const float g = G[s];
  cf t1[3], t2[3], t3[3];
#pragma unroll
  for (int i = 0; i < 2; ++i) twk[i] = tw_get(4 * (kk + 64 * i));
#pragma unroll
  for (int k = 1; k < 4; ++k) {
    t1[k - 1] = tw_get(8 * lane * k);
    t2[k - 1] = tw_get(32 * (lane & 15) * k);
    t3[k - 1] = tw_get(128 * (lane & 3) * k);
  }
  cf* zr = smem + sr * R3;
#pragma unroll
  for (int i = 0; i < 2; ++i) {
    const int k = kk + 64 * i;
    if (k == 0) {
      zr[0] = mk(ya[i].x + ya[i].y, ya[i].x - ya[i].y);
      zr[128] = mk(2.f * yb[i].x, -2.f * yb[i].y);
    } else {
      const cf A = mk(ya[i].x + yb[i].x, ya[i].y - yb[i].y);
      const cf Bv = mk(ya[i].x - yb[i].x, ya[i].y + yb[i].y);
      const cf wb = cmulc(Bv, twk[i]);
      const cf T = mk(-wb.y, wb.x);
      zr[k] = cadd(A, T);
      zr[256 - k] = mk(A.x - T.x, T.y - A.y);
    }
  }
  __syncthreads();
  cf* sc = smem + wv * R3;
  cf v[4];
  {
    const int kb = (lane >> 4) + 4 * ((lane >> 2) & 3) + 16 * (lane & 3);
#pragma unroll
    for (int l = 0; l < 4; ++l) v[l] = sc[kb + 64 * l];
  }
  wave_sync();
  dft4<1>(v[0], v[1], v[2], v[3]);
#pragma unroll
  for (int p2 = 0; p2 < 4; ++p2) sc[(lane & 3) * 65 + (lane & ~3) + p2] = v[p2];
  wave_sync();
#pragma unroll
  for (int i1 = 0; i1 < 4; ++i1) v[i1] = sc[i1 * 65 + lane];
  wave_sync();
#pragma unroll
  for (int k = 1; k < 4; ++k) v[k] = cmulc(v[k], t3[k - 1]);
  dft4<1>(v[0], v[1], v[2], v[3]);
#pragma unroll
  for (int p1 = 0; p1 < 4; ++p1) sc[(lane >> 2) * 20 + 4 * p1 + (lane & 3)] = v[p1];
  wave_sync();
#pragma unroll
  for (int j1 = 0; j1 < 4; ++j1) v[j1] = sc[((lane >> 4) * 4 + j1) * 20 + (lane & 15)];
  wave_sync();
#pragma unroll
  for (int k = 1; k < 4; ++k) v[k] = cmulc(v[k], t2[k - 1]);
  dft4<1>(v[0], v[1], v[2], v[3]);
#pragma unroll
  for (int m1 = 0; m1 < 4; ++m1) sc[(lane >> 4) * 80 + 16 * m1 + (lane & 15)] = v[m1];
  wave_sync();
#pragma unroll
  for (int k1 = 0; k1 < 4; ++k1) v[k1] = sc[k1 * 80 + lane];
#pragma unroll
  for (int k = 1; k < 4; ++k) v[k] = cmulc(v[k], t1[k - 1]);
  dft4<1>(v[0], v[1], v[2], v[3]);
  const float sc20 = 1.f / 1048576.f;
  const float og = rbf(1.f - g);
  unsigned* orow = reinterpret_cast<unsigned*>(out + (size_t)s * D_);
#pragma unroll
  for (int r = 0; r < 4; ++r) {
    const unsigned ap = pk_bf16(v[r].x * sc20, v[r].y * sc20);
    const unsigned ga = pk_bf16(g * lo_f(ap), g * hi_f(ap));
    const unsigned gb = pk_bf16(og * lo_f(bv[r]), og * hi_f(bv[r]));
    orow[lane + 64 * r] = pk_bf16(lo_f(ga) + lo_f(gb), hi_f(ga) + hi_f(gb));
  }
  TRACE_END(ws, 2)
}

#ifdef POP_TARGET_ATTR
#pragma clang attribute pop
#endif

extern "C" void kernel_launch(void* const* d_in, const int* in_sizes, int n_in, void* d_out, int out_size, void* d_ws,
                              size_t ws_size, hipStream_t stream) {
  const unsigned short* x = (const unsigned short*)d_in[0];
  const unsigned short* wr = (const unsigned short*)d_in[1];
  const unsigned short* wi = (const unsigned short*)d_in[2];
  const unsigned short* wg = (const unsigned short*)d_in[4];
  const unsigned short* wu = (const unsigned short*)d_in[5];
  const unsigned short* wd = (const unsigned short*)d_in[6];
  const unsigned short* gw = (const unsigned short*)d_in[7];
  char* ws = (char*)d_ws;
  (void)hipFuncSetAttribute((const void*)k1_kernel, hipFuncAttributeMaxDynamicSharedMemorySize, K1_LDS);
  (void)hipFuncSetAttribute((const void*)k2_kernel, hipFuncAttributeMaxDynamicSharedMemorySize, K2_LDS);
  hipLaunchKernelGGL(k1_kernel, dim3(K1_GRID), dim3(256), K1_LDS, stream, x, wr, wi, wg, wu, gw, ws);
  hipLaunchKernelGGL(k2_kernel, dim3(K2_GRID), dim3(256), K2_LDS, stream, wd, ws);
  hipLaunchKernelGGL(k3_kernel, dim3(S_ / 8), dim3(512), 0, stream, (const char*)ws, (unsigned short*)d_out);
}
```

```cpp
#if defined(__HIP_DEVICE_COMPILE__) && __HIP_DEVICE_COMPILE__
#pragma clang attribute push(__attribute__((target("no-packed-fp32-ops"))), apply_to = function)
#define POP_TARGET_ATTR 1
#endif
#define ROT1 1
#define ROT2 0
#define PFWD 7
#define AUXFIRST 1
#define PRIO1L 1
#include <hip/hip_runtime.h>
#include <hip/hip_bf16.h>
#include <stdint.h>
#define TID_X ((int)__builtin_amdgcn_workitem_id_x())
#define BID_X ((int)__builtin_amdgcn_workgroup_id_x())

typedef short bf16x8 __attribute__((ext_vector_type(8)));
typedef short s16x4 __attribute__((ext_vector_type(4)));
typedef float f32x2 __attribute__((ext_vector_type(2)));
typedef float f32x4 __attribute__((ext_vector_type(4)));
typedef float f32x16 __attribute__((ext_vector_type(16)));
typedef unsigned u32x4 __attribute__((ext_vector_type(4)));
typedef unsigned u32x2 __attribute__((ext_vector_type(2)));
typedef __bf16 bf16x2_t __attribute__((ext_vector_type(2)));
typedef __attribute__((address_space(3))) s16x4* lds_s16x4_ptr;
typedef __attribute__((address_space(3))) void* lds_void_ptr;
typedef const __attribute__((address_space(1))) void* glb_cvoid_ptr;

#define DI __device__ __forceinline__
#define SB() __builtin_amdgcn_sched_barrier(0)

constexpr int S_ = 2048, D_ = 512, F_ = 1376, FP_ = 1408, DF_ = 257;
constexpr int XS_ = 2080;

constexpr size_t WS_XT = 0;
constexpr size_t WS_H = 5u << 20;
constexpr size_t WS_B = 11u << 20;
constexpr size_t WS_WT = 13u << 20;
constexpr size_t WS_G = 16u << 20;
constexpr size_t WS_YT = 17u << 20;

#define TRACE_BEGIN
#define TRACE_END(ws, k)

struct cf { float x, y; };
DI cf mk(float x, float y) { cf r; r.x = x; r.y = y; return r; }

DI cf tw_get(int j) {
  const float r = (float)(j & 2047) * (1.f / 2048.f);
  return mk(__builtin_amdgcn_cosf(r), -__builtin_amdgcn_sinf(r));
}
DI unsigned pk_bf16(float a, float b) {
  f32x2 v = {a, b};
  return __builtin_bit_cast(unsigned, __builtin_convertvector(v, bf16x2_t));
}
DI float lo_f(unsigned w) { return __builtin_bit_cast(float, w << 16); }
DI float hi_f(unsigned w) { return __builtin_bit_cast(float, w & 0xffff0000u); }
DI float rbf(float f) { return lo_f(pk_bf16(f, f)); }
DI float sigm(float z) { return __builtin_amdgcn_rcpf(1.f + __builtin_amdgcn_exp2f(-1.4426950408889634f * z)); }

DI cf cadd(cf a, cf b) { return mk(a.x + b.x, a.y + b.y); }
DI cf csub(cf a, cf b) { return mk(a.x - b.x, a.y - b.y); }
DI cf cmul(cf a, cf b) { return mk(a.x * b.x - a.y * b.y, a.x * b.y + a.y * b.x); }
DI cf cmulc(cf a, cf b) { return mk(a.x * b.x + a.y * b.y, a.y * b.x - a.x * b.y); }
template <int SGN> DI cf mul_i(cf a) { return SGN < 0 ? mk(a.y, -a.x) : mk(-a.y, a.x); }

template <int SGN> DI void dft4(cf& b0, cf& b1, cf& b2, cf& b3) {
  cf c0 = cadd(b0, b2), c1 = csub(b0, b2), c2 = cadd(b1, b3), c3 = mul_i<SGN>(csub(b1, b3));
  b0 = cadd(c0, c2); b2 = csub(c0, c2); b1 = cadd(c1, c3); b3 = csub(c1, c3);
}
template <int SGN> DI void dft8(cf* v) {
  const float r = 0.70710678118654752440f;
  cf a0 = cadd(v[0], v[4]), a4 = csub(v[0], v[4]);
  cf a1 = cadd(v[1], v[5]), a5 = csub(v[1], v[5]);
  cf a2 = cadd(v[2], v[6]), a6 = csub(v[2], v[6]);
  cf a3 = cadd(v[3], v[7]), a7 = csub(v[3], v[7]);
  if (SGN < 0) {
    a5 = mk((a5.x + a5.y) * r, (a5.y - a5.x) * r);
    a7 = mk((a7.y - a7.x) * r, -(a7.x + a7.y) * r);
  } else {
    a5 = mk((a5.x - a5.y) * r, (a5.y + a5.x) * r);
    a7 = mk(-(a7.x + a7.y) * r, (a7.x - a7.y) * r);
  }
  a6 = mul_i<SGN>(a6);
  dft4<SGN>(a0, a1, a2, a3);
  dft4<SGN>(a4, a5, a6, a7);
  v[0] = a0; v[2] = a1; v[4] = a2; v[6] = a3;
  v[1] = a4; v[3] = a5; v[5] = a6; v[7] = a7;
}

DI void wave_sync() {
  __builtin_amdgcn_fence(__ATOMIC_RELEASE, "wavefront");
  __builtin_amdgcn_wave_barrier();
  __builtin_amdgcn_fence(__ATOMIC_ACQUIRE, "wavefront");
}

constexpr int RGN = 292;

DI void fft256_fwd(cf* v, cf* sc, int j, const cf* tw1, const cf* tw2) {
  dft8<-1>(v);
#pragma unroll
  for (int k = 1; k < 8; ++k) v[k] = cmul(v[k], tw1[k - 1]);
#pragma unroll
  for (int k = 0; k < 8; ++k) sc[k * 36 + j] = v[k];
  wave_sync();
  const int k1 = j >> 2, a = j & 3;
#pragma unroll
  for (int m = 0; m < 8; ++m) v[m] = sc[k1 * 36 + 4 * m + a];
  wave_sync();
  dft8<-1>(v);
#pragma unroll
  for (int k = 1; k < 8; ++k) v[k] = cmul(v[k], tw2[k - 1]);
#pragma unroll
  for (int k = 0; k < 8; ++k) sc[k * 33 + j] = v[k];
  wave_sync();
#pragma unroll
  for (int e = 0; e < 2; ++e)
#pragma unroll
    for (int m = 0; m < 4; ++m) v[e * 4 + m] = sc[(a + 4 * e) * 33 + k1 * 4 + m];
  wave_sync();
  dft4<-1>(v[0], v[1], v[2], v[3]);
  dft4<-1>(v[4], v[5], v[6], v[7]);
}
DI void fft256_inv(cf* v, cf* sc, int j, const cf* tw1, const cf* tw2) {
  const int k1 = j >> 2, a = j & 3;
  dft4<1>(v[0], v[1], v[2], v[3]);
  dft4<1>(v[4], v[5], v[6], v[7]);
#pragma unroll
  for (int e = 0; e < 2; ++e)
#pragma unroll
    for (int m = 0; m < 4; ++m) sc[(a + 4 * e) * 33 + k1 * 4 + m] = v[e * 4 + m];
  wave_sync();
#pragma unroll
  for (int k = 0; k < 8; ++k) v[k] = sc[k * 33 + j];
  wave_sync();
#pragma unroll
  for (int k = 1; k < 8; ++k) v[k] = cmulc(v[k], tw2[k - 1]);
  dft8<1>(v);
#pragma unroll
  for (int m = 0; m < 8; ++m) sc[k1 * 36 + 4 * m + a] = v[m];
  wave_sync();
#pragma unroll
  for (int k = 0; k < 8; ++k) v[k] = sc[k * 36 + j];
  wave_sync();
#pragma unroll
  for (int k = 1; k < 8; ++k) v[k] = cmulc(v[k], tw1[k - 1]);
  dft8<1>(v);
}
DI void load_tw256(int j, cf* tw1, cf* tw2) {
#pragma unroll
  for (int k = 1; k < 8; ++k) tw1[k - 1] = tw_get(8 * j * k);
#pragma unroll
  for (int k = 1; k < 8; ++k) tw2[k - 1] = tw_get(64 * (j & 3) * k);
}

#define SM_H 0
#define SM_X 0
#define SM_Y 0
#define SM_B 0
template <int MODE> DI void st16(void* p, u32x4 v) {
  if (MODE == 0) *reinterpret_cast<u32x4*>(p) = v;
  else asm volatile("global_store_dwordx4 %0, %1, off sc1\n\ts_nop 1" ::"v"(p), "v"(v) : "memory");
}
template <int MODE> DI void st8(void* p, u32x2 v) {
  if (MODE == 0) *reinterpret_cast<u32x2*>(p) = v;
  else asm volatile("global_store_dwordx2 %0, %1, off sc1" ::"v"(p), "v"(v) : "memory");
}
DI u32x4 f4u(f32x4 v) { return __builtin_bit_cast(u32x4, v); }

DI void glds16(const void* g, char* lds) {
  __builtin_amdgcn_global_load_lds((glb_cvoid_ptr)g, (lds_void_ptr)lds, 16, 0, 0);
}

constexpr int G1_TILES = 240;
constexpr int AUX_WG = 256;
constexpr int K1_GRID = G1_TILES + AUX_WG;
constexpr int K1_LDS = 81920;

DI void gemm1_role(char* smem, int b, const unsigned short* __restrict__ x, const unsigned short* __restrict__ wg,
                   const unsigned short* __restrict__ wu, unsigned short* __restrict__ H) {
  const int tid = TID_X, lane = tid & 63, wid = tid >> 6;
  const int xcd = b & 7, li = b >> 3;
  const int ft = li >> 1, mt = xcd * 2 + (li & 1);
  const int m0 = mt * 128, f0 = ft * 96;
  char* const As = smem;
  char* const Bs = smem + 32768;

  const unsigned short* asrc[2];
#pragma unroll
  for (int i = 0; i < 2; ++i) {
    const int row = (wid * 2 + i) * 16 + (lane >> 2), p = lane & 3;
    asrc[i] = x + (size_t)(m0 + row) * D_ + (p ^ ((4 - ((row >> 2) & 3)) & 3)) * 8;
  }
  const unsigned short* bsrc[3];
#pragma unroll
  for (int i = 0; i < 3; ++i) {
    const int n16 = 4 * i + (lane >> 4);
    const int rp = (lane >> 1) & 7, hf = lane & 1;
    const int k = wid * 8 + (rp ^ (4 * (wid & 1)));
    int col = f0 + (n16 % 6) * 16 + hf * 8;
    col = col > F_ - 8 ? F_ - 8 : col;
    bsrc[i] = (n16 < 6 ? wg : wu) + (size_t)k * F_ + col;
  }
  const int rot = ROT1 ? __builtin_amdgcn_readfirstlane(2 * ((xcd + ft) & 7)) : 0;
  auto glA = [&](int tt, int i) { glds16(asrc[i] + ((tt + rot) & 15) * 32, As + (tt & 3) * 8192 + (wid * 2 + i) * 1024); };
  auto glB = [&](int tt, int i) { glds16(bsrc[i] + (size_t)((tt + rot) & 15) * 32 * F_, Bs + (tt & 3) * 12288 + (wid * 3 + i) * 1024); };

  const int wm = wid >> 1, wn = wid & 1;
  const int g = lane >> 4, q = (lane & 15) >> 2, p = lane & 3;
  int aoff[4];
#pragma unroll
  for (int i = 0; i < 4; ++i) {
    const int row = wm * 64 + i * 16 + (lane & 15);
    aoff[i] = row * 64 + ((g ^ ((4 - ((row >> 2) & 3)) & 3)) << 4);
  }
  const int bo0 = (g * 12 + wn * 3) * 256 + ((q ^ (4 * (g & 1))) * 32) + p * 8;
  const int bo1 = (g * 12 + wn * 3) * 256 + (((4 + q) ^ (4 * (g & 1))) * 32) + p * 8;

  f32x4 acc[4][6];
#pragma unroll
  for (int i = 0; i < 4; ++i)
#pragma unroll
    for (int j = 0; j < 6; ++j) acc[i][j] = (f32x4){0.f, 0.f, 0.f, 0.f};

  bf16x8 af[2][4];
  s16x4 bl[2][6], bh[2][6];
  auto dsr = [&](int t, int set, int n) {
    const char* ab = As + (t & 3) * 8192;
    const char* bb = Bs + (t & 3) * 12288;
    if (n < 12) {
      const int j = n >> 1, o = (j < 3 ? j : j + 3) * 256;
      if (n & 1) bh[set][j] = __builtin_amdgcn_ds_read_tr16_b64_v4i16((lds_s16x4_ptr)(bb + bo1 + o));
      else bl[set][j] = __builtin_amdgcn_ds_read_tr16_b64_v4i16((lds_s16x4_ptr)(bb + bo0 + o));
    } else {
      af[set][n - 12] = *reinterpret_cast<const bf16x8*>(ab + aoff[n - 12]);
    }
  };
  auto gl = [&](int t, int gi) {
    if (t & 1) {
      if (gi < 4) { glA(t + 3 + (gi & 1), gi >> 1); return; }
      gi -= 4;
    }
    if (gi < 3) glB(t + 4, gi);
  };
  auto mm = [&](int set, int i) {
    const int mi = i / 6, j = i % 6;
    acc[mi][j] = __builtin_amdgcn_mfma_f32_16x16x32_bf16(__builtin_shufflevector(bl[set][j], bh[set][j], 0, 1, 2, 3, 4, 5, 6, 7),
                                                         af[set][mi], acc[mi][j], 0, 0, 0);
  };

  glA(0, 0); glA(1, 0); glA(0, 1); glA(1, 1);
  glB(0, 0); glB(0, 1); glB(0, 2); glB(1, 0); glB(1, 1); glB(1, 2);
  glA(2, 0); glA(3, 0); glA(2, 1); glA(3, 1);
  glB(2, 0); glB(2, 1); glB(2, 2); glB(3, 0); glB(3, 1); glB(3, 2);
  __builtin_amdgcn_s_setprio(PRIO1L);
  asm volatile("s_waitcnt vmcnt(13)\n\ts_barrier" ::: "memory");
#pragma unroll
  for (int n = 0; n < 16; ++n) dsr(0, 0, n);
  SB();
#pragma unroll
  for (int t = 0; t < 16; ++t) {
    if (t < 15) {
      if (t == 14) asm volatile("s_waitcnt vmcnt(0) lgkmcnt(0)\n\ts_barrier" ::: "memory");
      else if (t == 13) asm volatile("s_waitcnt vmcnt(3) lgkmcnt(0)\n\ts_barrier" ::: "memory");
      else if (t & 1) asm volatile("s_waitcnt vmcnt(6) lgkmcnt(0)\n\ts_barrier" ::: "memory");
      else asm volatile("s_waitcnt vmcnt(10) lgkmcnt(0)\n\ts_barrier" ::: "memory");
    }
    SB();
#pragma unroll
    for (int i = 0; i < 24; ++i) {
      mm(t & 1, i);
      SB();
      if (t < 15 && i < 16) { dsr(t + 1, (t + 1) & 1, i); SB(); }
      if (t + 4 < 16 && (i % 3) == 2) {
        const int gi = i / 3;
        if (gi < ((t & 1) ? 7 : 3)) { gl(t, gi); SB(); }
      }
    }
  }
  asm volatile("s_waitcnt vmcnt(0) lgkmcnt(0)\n\ts_barrier" ::: "memory");

  constexpr int HS = 56;
  unsigned short* tl = reinterpret_cast<unsigned short*>(smem) + wid * (64 * HS);
#pragma unroll
  for (int hf = 0; hf < 2; ++hf) {
#pragma unroll
    for (int m2 = 0; m2 < 2; ++m2)
#pragma unroll
      for (int j = 0; j < 3; ++j) {
        const int mi = hf * 2 + m2;
        u32x2 o;
#pragma unroll
        for (int e = 0; e < 2; ++e) {
          const unsigned gp = pk_bf16(acc[mi][j][2 * e], acc[mi][j][2 * e + 1]);
          const unsigned up = pk_bf16(acc[mi][j + 3][2 * e], acc[mi][j + 3][2 * e + 1]);
          const float g0 = lo_f(gp), g1 = hi_f(gp);
          const unsigned sp = pk_bf16(g0 * sigm(g0), g1 * sigm(g1));
          o[e] = pk_bf16(lo_f(sp) * lo_f(up), hi_f(sp) * hi_f(up));
        }
        *reinterpret_cast<u32x2*>(tl + (mi * 16 + (lane & 15)) * HS + j * 16 + g * 4) = o;
      }
    wave_sync();
#pragma unroll
    for (int i = 0; i < 3; ++i) {
      const int c = lane + 64 * i, row = hf * 32 + c / 6, ch = c % 6;
      const int f = f0 + wn * 48 + ch * 8;
      if (f < FP_) {
        u32x4 v = *reinterpret_cast<const u32x4*>(tl + row * HS + ch * 8);
        if (f >= F_) v = (u32x4){0u, 0u, 0u, 0u};
        st16<SM_H>(H + (size_t)(m0 + wm * 64 + row) * FP_ + f, v);
      }
    }
    SB();
  }
}

DI void aux1_role(char* smem, int b, const unsigned short* __restrict__ x, const unsigned short* __restrict__ gw,
                  const unsigned short* __restrict__ wr, const unsigned short* __restrict__ wi, cf* __restrict__ XT,
                  float* __restrict__ G, unsigned* __restrict__ WT, const unsigned short* __restrict__ wd,
                  const unsigned short* __restrict__ wg, const unsigned short* __restrict__ wu) {
  const int tid = TID_X, j = tid & 31, hw = tid >> 5;
  unsigned pf = 0;
  if (PFWD && tid < 192) {
    const int m = tid >> 6, l = tid & 63;
    if (l < 43 && ((PFWD >> m) & 1)) {
      const unsigned short* wsel = m == 0 ? wg : (m == 1 ? wu : wd);
      pf = reinterpret_cast<const unsigned*>(wsel)[((size_t)b * 43 + l) * 32];
    }
  }
  const int s0 = b * 8, s = s0 + hw;
  cf* sc = reinterpret_cast<cf*>(smem) + hw * RGN;
  const unsigned* xr = reinterpret_cast<const unsigned*>(x + (size_t)s * D_);
  const unsigned* gr = reinterpret_cast<const unsigned*>(gw);
  unsigned xv[8], gv[8];
#pragma unroll
  for (int r = 0; r < 8; ++r) xv[r] = xr[j + 32 * r];
#pragma unroll
  for (int r = 0; r < 8; ++r) gv[r] = gr[j + 32 * r];
  const u32x4* sr4 = reinterpret_cast<const u32x4*>(wr + (size_t)s0 * DF_);
  const u32x4* si4 = reinterpret_cast<const u32x4*>(wi + (size_t)s0 * DF_);
  const u32x4 w0 = sr4[tid], w1 = si4[tid];
  u32x4 w2 = w0;
  if (tid < 2) w2 = (tid == 0 ? sr4 : si4)[256];
  cf tw1[7], tw2[7];
  load_tw256(j, tw1, tw2);
  u32x4* lw = reinterpret_cast<u32x4*>(smem + 20480);
  lw[tid] = w0; lw[258 + tid] = w1;
  if (tid < 2) lw[tid * 258 + 256] = w2;

  cf v[8];
  float gs = 0.f;
#pragma unroll
  for (int r = 0; r < 8; ++r) {
    v[r] = mk(lo_f(xv[r]), hi_f(xv[r]));
    const unsigned pp = pk_bf16(v[r].x * lo_f(gv[r]), v[r].y * hi_f(gv[r]));
    gs += lo_f(pp);
    gs += hi_f(pp);
  }
#pragma unroll
  for (int o = 16; o > 0; o >>= 1) gs += __shfl_xor(gs, o);
  if (j == 0) G[s] = rbf(sigm(rbf(gs)));
  fft256_fwd(v, sc, j, tw1, tw2);
  {
    const int k1 = j >> 2, a = j & 3;
#pragma unroll
    for (int e = 0; e < 2; ++e)
#pragma unroll
      for (int l = 0; l < 4; ++l) sc[k1 + 8 * (a + 4 * e) + 64 * l] = v[e * 4 + l];
  }
  __syncthreads();
  {
    const int sp = (tid & 3) * 2, kk = tid >> 2;
    const cf* z0 = reinterpret_cast<const cf*>(smem) + sp * RGN;
    const cf* z1 = z0 + RGN;
#pragma unroll
    for (int i = 0; i < 2; ++i) {
      const int k = kk + 64 * i;
      if (k == 0) {
        const cf a0 = z0[0], ah = z0[128], b0 = z1[0], bh = z1[128];
        st16<SM_X>(XT + s0 + sp, f4u((f32x4){a0.x + a0.y, a0.x - a0.y, b0.x + b0.y, b0.x - b0.y}));
        st16<SM_X>(XT + (size_t)128 * XS_ + s0 + sp, f4u((f32x4){ah.x, -ah.y, bh.x, -bh.y}));
      } else {
        const cf w = tw_get(4 * k);
        f32x4 o0, o1;
#pragma unroll
        for (int rr = 0; rr < 2; ++rr) {
          const cf za = (rr ? z1 : z0)[k], zb = (rr ? z1 : z0)[256 - k];
          const cf E = mk(0.5f * (za.x + zb.x), 0.5f * (za.y - zb.y));
          const cf O = mk(0.5f * (za.y + zb.y), 0.5f * (zb.x - za.x));
          const cf wo = cmul(O, w);
          o0[2 * rr] = E.x + wo.x; o0[2 * rr + 1] = E.y + wo.y;
          o1[2 * rr] = E.x - wo.x; o1[2 * rr + 1] = wo.y - E.y;
        }
        st16<SM_X>(XT + (size_t)k * XS_ + s0 + sp, f4u(o0));
        st16<SM_X>(XT + (size_t)(256 - k) * XS_ + s0 + sp, f4u(o1));
      }
    }
  }
  {
    const unsigned short* fr = reinterpret_cast<const unsigned short*>(lw);
    const unsigned short* fi = reinterpret_cast<const unsigned short*>(lw + 258);
    const int r4 = (tid & 1) * 4;
#pragma unroll
    for (int it = 0; it < 3; ++it) {
      const int c = (tid >> 1) + 128 * it;
      if (c < DF_) {
        u32x4 o;
#pragma unroll
        for (int e = 0; e < 4; ++e) o[e] = (unsigned)fr[(r4 + e) * DF_ + c] | ((unsigned)fi[(r4 + e) * DF_ + c] << 16);
        st16<SM_X>(WT + (size_t)c * XS_ + s0 + r4, o);
      }
    }
  }
  asm volatile("" ::"v"(pf));
}

__global__ __launch_bounds__(256, 2) void k1_kernel(const unsigned short* __restrict__ x, const unsigned short* __restrict__ wr,
                                                    const unsigned short* __restrict__ wi, const unsigned short* __restrict__ wg,
                                                    const unsigned short* __restrict__ wu, const unsigned short* __restrict__ gw,
                                                    const unsigned short* __restrict__ wd, char* __restrict__ ws) {
  extern __shared__ __attribute__((aligned(16))) char smem[];
  const int b = BID_X;
  TRACE_BEGIN
  const int bg = b - AUX_WG, ba = b;
  if (b >= AUX_WG) {
    gemm1_role(smem, bg, x, wg, wu, reinterpret_cast<unsigned short*>(ws + WS_H));
  } else {
    aux1_role(smem, (ba & 7) * 32 + (ba >> 3), x, gw, wr, wi, reinterpret_cast<cf*>(ws + WS_XT), reinterpret_cast<float*>(ws + WS_G),
              reinterpret_cast<unsigned*>(ws + WS_WT), wd, wg, wu);
  }
  TRACE_END(ws, 0)
}

constexpr int G2_TILES = 256;
constexpr int K2_GRID = G2_TILES + 256;
constexpr int K2_LDS = 81920;

DI void gemm2_role(char* smem, int b, const unsigned short* __restrict__ H, const unsigned short* __restrict__ wd,
                   unsigned short* __restrict__ Bo) {
  const int tid = TID_X, lane = tid & 63, wid = tid >> 6;
  const int xcd = b & 7, li = b >> 3;
  const int mt = xcd * 4 + (li >> 3), nt = li & 7;
  const int m0 = mt * 64, n0 = nt * 64;
  const unsigned short* asrc[2];
  int bk[2];
#pragma unroll
  for (int i = 0; i < 2; ++i) {
    const int row = (wid * 2 + i) * 8 + (lane >> 3), c = (lane & 7) ^ ((row >> 1) & 7);
    asrc[i] = H + (size_t)(m0 + row) * FP_ + c * 8;
    bk[i] = (wid * 2 + i) * 8 + (((lane >> 1) & 7) ^ (4 * ((lane >> 4) & 1)));
  }
  const unsigned short* bsrc = wd + n0 + (lane >> 4) * 16 + (lane & 1) * 8;
  const int rot = ROT2 ? __builtin_amdgcn_readfirstlane((mt * 11) % 22 + (mt >> 1) % 11) % 22 : 0;
  auto gl = [&](int t, int i) {
    char* buf = smem + (t % 5) * 16384;
    int kt = t + rot;
    kt = kt >= 22 ? kt - 22 : kt;
    if (i < 2) glds16(asrc[i] + kt * 64, buf + (wid * 2 + i) * 1024);
    else {
      int k = kt * 64 + bk[i - 2];
      k = k < F_ ? k : F_ - 1;
      glds16(bsrc + (size_t)k * D_, buf + 8192 + (wid * 2 + i - 2) * 1024);
    }
  };
  const int wm = wid >> 1, wn = wid & 1;
  const int h = lane >> 5, gi = lane >> 4, q = (lane & 15) >> 2, p = lane & 3;
  const int arow = wm * 32 + (lane & 31);
  int aoff[4];
#pragma unroll
  for (int ks = 0; ks < 4; ++ks) aoff[ks] = arow * 128 + (((2 * ks + h) ^ ((arow >> 1) & 7)) << 4);
  const int bbase = 8192 + (h * 4 + wn * 2 + (gi & 1)) * 256 + p * 8;
  const int bo0 = bbase + ((q ^ (4 * (gi & 1))) * 32), bo1 = bbase + (((4 + q) ^ (4 * (gi & 1))) * 32);
  f32x16 acc, acc2;
#pragma unroll
  for (int r = 0; r < 16; ++r) { acc[r] = 0.f; acc2[r] = 0.f; }
  bf16x8 a[2][4];
  s16x4 bl[2][4], bh[2][4];
  auto dsr = [&](int t, int set, int ks) {
    const char* buf = smem + (t % 5) * 16384;
    a[set][ks] = *reinterpret_cast<const bf16x8*>(buf + aoff[ks]);
    bl[set][ks] = __builtin_amdgcn_ds_read_tr16_b64_v4i16((lds_s16x4_ptr)(buf + bo0 + ks * 2048));
    bh[set][ks] = __builtin_amdgcn_ds_read_tr16_b64_v4i16((lds_s16x4_ptr)(buf + bo1 + ks * 2048));
  };
  constexpr int NT = 22;
#pragma unroll
  for (int t = 0; t < 5; ++t)
#pragma unroll
    for (int i = 0; i < 4; ++i) gl(t, i);
#define PRIOG2L 1
  __builtin_amdgcn_s_setprio(PRIOG2L);
  asm volatile("s_waitcnt vmcnt(16)\n\ts_barrier" ::: "memory");
#pragma unroll
  for (int i = 0; i < 4; ++i) dsr(0, 0, i);
  SB();
#pragma unroll
  for (int t = 0; t < NT; ++t) {
    if (t < NT - 1) {
      const int later = (t + 4 < NT ? t + 4 : NT - 1) - (t + 1);
      if (later == 3) asm volatile("s_waitcnt vmcnt(12) lgkmcnt(0)\n\ts_barrier" ::: "memory");
      else if (later == 2) asm volatile("s_waitcnt vmcnt(8) lgkmcnt(0)\n\ts_barrier" ::: "memory");
      else if (later == 1) asm volatile("s_waitcnt vmcnt(4) lgkmcnt(0)\n\ts_barrier" ::: "memory");
      else asm volatile("s_waitcnt vmcnt(0) lgkmcnt(0)\n\ts_barrier" ::: "memory");
    }
    SB();
#pragma unroll
    for (int i = 0; i < 4; ++i) {
      if (i & 1) acc2 = __builtin_amdgcn_mfma_f32_32x32x16_bf16(__builtin_shufflevector(bl[t & 1][i], bh[t & 1][i], 0, 1, 2, 3, 4, 5, 6, 7),
                                                                a[t & 1][i], acc2, 0, 0, 0);
      else acc = __builtin_amdgcn_mfma_f32_32x32x16_bf16(__builtin_shufflevector(bl[t & 1][i], bh[t & 1][i], 0, 1, 2, 3, 4, 5, 6, 7),
                                                         a[t & 1][i], acc, 0, 0, 0);
      SB();
      if (t < NT - 1) { dsr(t + 1, (t + 1) & 1, i); SB(); }
      if (t + 5 < NT) { gl(t + 5, i); SB(); }
    }
  }
#pragma unroll
  for (int r = 0; r < 16; ++r) acc[r] += acc2[r];
  unsigned short* drow = Bo + (size_t)(m0 + arow) * D_ + n0 + wn * 32 + h * 8;
#pragma unroll
  for (int rp = 0; rp < 2; ++rp) {
    unsigned e0 = pk_bf16(acc[8 * rp], acc[8 * rp + 1]), e1 = pk_bf16(acc[8 * rp + 2], acc[8 * rp + 3]);
    unsigned o0 = pk_bf16(acc[8 * rp + 4], acc[8 * rp + 5]), o1 = pk_bf16(acc[8 * rp + 6], acc[8 * rp + 7]);
    auto r0 = __builtin_amdgcn_permlane32_swap(e0, o0, false, false);
    auto r1 = __builtin_amdgcn_permlane32_swap(e1, o1, false, false);
    st16<SM_B>(drow + rp * 16, (u32x4){r0[0], r1[0], r0[1], r1[1]});
  }
}

DI int col_u(int t, int slot) {
  const int j = t & 31;
  return (t >> 5) + 8 * ((j >> 2) + 8 * ((j & 3) + 4 * (slot >> 2)) + 64 * (slot & 3));
}

DI void fft2_role(char* smem, int col, const cf* __restrict__ XT, const unsigned* __restrict__ WT, cf* __restrict__ YT) {
  const int t = TID_X, j = t & 31, hw = t >> 5;
  cf* all = reinterpret_cast<cf*>(smem);
  cf* sc = all + hw * RGN;
  const cf* xc = XT + (size_t)col * XS_;
  cf v[8];
#pragma unroll
  for (int r = 0; r < 8; ++r) v[r] = xc[t + 256 * r];
  unsigned w[8], wm[8], wn[8], wnm[8];
#pragma unroll
  for (int r = 0; r < 8; ++r) w[r] = WT[(size_t)col * XS_ + col_u(t, r)];
  if (col == 0) {
#pragma unroll
    for (int r = 0; r < 8; ++r) {
      const int u = col_u(t, r), um = (S_ - u) & (S_ - 1);
      wm[r] = WT[um]; wn[r] = WT[(size_t)256 * XS_ + u]; wnm[r] = WT[(size_t)256 * XS_ + um];
    }
  }
  cf tw0[7], tw1[7], tw2[7];
#pragma unroll
  for (int k = 1; k < 8; ++k) tw0[k - 1] = tw_get(t * k);
  load_tw256(j, tw1, tw2);

  dft8<-1>(v);
#pragma unroll
  for (int k = 1; k < 8; ++k) v[k] = cmul(v[k], tw0[k - 1]);
#pragma unroll
  for (int k = 0; k < 8; ++k) all[k * RGN + t] = v[k];
  __syncthreads();
#pragma unroll
  for (int r = 0; r < 8; ++r) v[r] = sc[j + 32 * r];
  wave_sync();
  fft256_fwd(v, sc, j, tw1, tw2);

  if (col != 0) {
#pragma unroll
    for (int r = 0; r < 8; ++r) v[r] = cmul(v[r], mk(lo_f(w[r]), hi_f(w[r])));
  } else {
    cf* nat = all + 8 * RGN;
#pragma unroll
    for (int r = 0; r < 8; ++r) { const int u = col_u(t, r); nat[(u & 7) * 260 + (u >> 3)] = v[r]; }
    __syncthreads();
#pragma unroll
    for (int r = 0; r < 8; ++r) {
      const int um = (S_ - col_u(t, r)) & (S_ - 1);
      const cf P = v[r], M = nat[(um & 7) * 260 + (um >> 3)];
      const cf X0 = mk(0.5f * (P.x + M.x), 0.5f * (P.y - M.y));
      const cf XN = mk(0.5f * (P.y + M.y), 0.5f * (M.x - P.x));
      const cf W0 = mk(0.5f * (lo_f(w[r]) + lo_f(wm[r])), 0.5f * (hi_f(w[r]) - hi_f(wm[r])));
      const cf WN = mk(0.5f * (lo_f(wn[r]) + lo_f(wnm[r])), 0.5f * (hi_f(wn[r]) - hi_f(wnm[r])));
      const cf Z0 = cmul(X0, W0), ZN = cmul(XN, WN);
      v[r] = mk(Z0.x - ZN.y, Z0.y + ZN.x);
    }
  }

  fft256_inv(v, sc, j, tw1, tw2);
#pragma unroll
  for (int r = 0; r < 8; ++r) sc[j + 32 * r] = v[r];
  __syncthreads();
#pragma unroll
  for (int k = 0; k < 8; ++k) v[k] = all[k * RGN + t];
#pragma unroll
  for (int k = 1; k < 8; ++k) v[k] = cmulc(v[k], tw0[k - 1]);
  dft8<1>(v);
#pragma unroll
  for (int r = 0; r < 8; ++r)
    st8<SM_Y>(YT + ((size_t)((t >> 3) + 32 * r) * 256 + col) * 8 + (t & 7), (u32x2){__builtin_bit_cast(unsigned, v[r].x), __builtin_bit_cast(unsigned, v[r].y)});
}

__global__ __launch_bounds__(256, 2) void k2_kernel(const unsigned short* __restrict__ wd, char* __restrict__ ws) {
  extern __shared__ __attribute__((aligned(16))) char smem[];
  const int b = BID_X;
  TRACE_BEGIN
  if (b >= 256) {
    gemm2_role(smem, b - 256, reinterpret_cast<const unsigned short*>(ws + WS_H), wd, reinterpret_cast<unsigned short*>(ws + WS_B));
  } else {
    fft2_role(smem, (b & 7) * 32 + (b >> 3), reinterpret_cast<const cf*>(ws + WS_XT), reinterpret_cast<const unsigned*>(ws + WS_WT),
              reinterpret_cast<cf*>(ws + WS_YT));
  }
  TRACE_END(ws, 1)
}

constexpr int R3 = 322;
__global__ __launch_bounds__(512) void k3_kernel(const char* __restrict__ ws, unsigned short* __restrict__ out) {
  __shared__ __attribute__((aligned(16))) cf smem[8 * R3];
  TRACE_BEGIN
  const int tid = TID_X, lane = tid & 63, wv = tid >> 6;
  const int blk = (BID_X & 7) * 32 + (BID_X >> 3);
  const int s0 = blk * 8, s = s0 + wv;
  const cf* YT = reinterpret_cast<const cf*>(ws + WS_YT) + (size_t)blk * 2048;
  const unsigned* Bo = reinterpret_cast<const unsigned*>(ws + WS_B) + (size_t)s * (D_ / 2);
  const float* G = reinterpret_cast<const float*>(ws + WS_G);
  const int sr = tid & 7, kk = tid >> 3;
  cf ya[2], yb[2], twk[2];
#pragma unroll
  for (int i = 0; i < 2; ++i) {
    const int k = kk + 64 * i;
    ya[i] = YT[k * 8 + sr];
    yb[i] = YT[(k == 0 ? 128 : 256 - k) * 8 + sr];
  }
  unsigned bv[4];
#pragma unroll
  for (int r = 0; r < 4; ++r) bv[r] = Bo[lane + 64 * r];
  const float g = G[s];
  cf t1[3], t2[3], t3[3];
#pragma unroll
  for (int i = 0; i < 2; ++i) twk[i] = tw_get(4 * (kk + 64 * i));
#pragma unroll
  for (int k = 1; k < 4; ++k) {
    t1[k - 1] = tw_get(8 * lane * k);
    t2[k - 1] = tw_get(32 * (lane & 15) * k);
    t3[k - 1] = tw_get(128 * (lane & 3) * k);
  }
  cf* zr = smem + sr * R3;
#pragma unroll
  for (int i = 0; i < 2; ++i) {
    const int k = kk + 64 * i;
    if (k == 0) {
      zr[0] = mk(ya[i].x + ya[i].y, ya[i].x - ya[i].y);
      zr[128] = mk(2.f * yb[i].x, -2.f * yb[i].y);
    } else {
      const cf A = mk(ya[i].x + yb[i].x, ya[i].y - yb[i].y);
      const cf Bv = mk(ya[i].x - yb[i].x, ya[i].y + yb[i].y);
      const cf wb = cmulc(Bv, twk[i]);
      const cf T = mk(-wb.y, wb.x);
      zr[k] = cadd(A, T);
      zr[256 - k] = mk(A.x - T.x, T.y - A.y);
    }
  }
  __syncthreads();
  cf* sc = smem + wv * R3;
  cf v[4];
  {
    const int kb = (lane >> 4) + 4 * ((lane >> 2) & 3) + 16 * (lane & 3);
#pragma unroll
    for (int l = 0; l < 4; ++l) v[l] = sc[kb + 64 * l];
  }
  wave_sync();
  dft4<1>(v[0], v[1], v[2], v[3]);
#pragma unroll
  for (int p2 = 0; p2 < 4; ++p2) sc[(lane & 3) * 65 + (lane & ~3) + p2] = v[p2];
  wave_sync();
#pragma unroll
  for (int i1 = 0; i1 < 4; ++i1) v[i1] = sc[i1 * 65 + lane];
  wave_sync();
#pragma unroll
  for (int k = 1; k < 4; ++k) v[k] = cmulc(v[k], t3[k - 1]);
  dft4<1>(v[0], v[1], v[2], v[3]);
#pragma unroll
  for (int p1 = 0; p1 < 4; ++p1) sc[(lane >> 2) * 20 + 4 * p1 + (lane & 3)] = v[p1];
  wave_sync();
#pragma unroll
  for (int j1 = 0; j1 < 4; ++j1) v[j1] = sc[((lane >> 4) * 4 + j1) * 20 + (lane & 15)];
  wave_sync();
#pragma unroll
  for (int k = 1; k < 4; ++k) v[k] = cmulc(v[k], t2[k - 1]);
  dft4<1>(v[0], v[1], v[2], v[3]);
#pragma unroll
  for (int m1 = 0; m1 < 4; ++m1) sc[(lane >> 4) * 80 + 16 * m1 + (lane & 15)] = v[m1];
  wave_sync();
#pragma unroll
  for (int k1 = 0; k1 < 4; ++k1) v[k1] = sc[k1 * 80 + lane];
#pragma unroll
  for (int k = 1; k < 4; ++k) v[k] = cmulc(v[k], t1[k - 1]);
  dft4<1>(v[0], v[1], v[2], v[3]);
  const float sc20 = 1.f / 1048576.f;
  const float og = rbf(1.f - g);
  unsigned* orow = reinterpret_cast<unsigned*>(out + (size_t)s * D_);
#pragma unroll
  for (int r = 0; r < 4; ++r) {
    const unsigned ap = pk_bf16(v[r].x * sc20, v[r].y * sc20);
    const unsigned ga = pk_bf16(g * lo_f(ap), g * hi_f(ap));
    const unsigned gb = pk_bf16(og * lo_f(bv[r]), og * hi_f(bv[r]));
    orow[lane + 64 * r] = pk_bf16(lo_f(ga) + lo_f(gb), hi_f(ga) + hi_f(gb));
  }
  TRACE_END(ws, 2)
}

#ifdef POP_TARGET_ATTR
#pragma clang attribute pop
#endif

extern "C" void kernel_launch(void* const* d_in, const int* in_sizes, int n_in, void* d_out, int out_size, void* d_ws,
                              size_t ws_size, hipStream_t stream) {
  const unsigned short* x = (const unsigned short*)d_in[0];
  const unsigned short* wr = (const unsigned short*)d_in[1];
  const unsigned short* wi = (const unsigned short*)d_in[2];
  const unsigned short* wg = (const unsigned short*)d_in[4];
  const unsigned short* wu = (const unsigned short*)d_in[5];
  const unsigned short* wd = (const unsigned short*)d_in[6];
  const unsigned short* gw = (const unsigned short*)d_in[7];
  char* ws = (char*)d_ws;
  (void)hipFuncSetAttribute((const void*)k1_kernel, hipFuncAttributeMaxDynamicSharedMemorySize, K1_LDS);
  (void)hipFuncSetAttribute((const void*)k2_kernel, hipFuncAttributeMaxDynamicSharedMemorySize, K2_LDS);
  hipLaunchKernelGGL(k1_kernel, dim3(K1_GRID), dim3(256), K1_LDS, stream, x, wr, wi, wg, wu, gw, wd, ws);
  hipLaunchKernelGGL(k2_kernel, dim3(K2_GRID), dim3(256), K2_LDS, stream, wd, ws);
  hipLaunchKernelGGL(k3_kernel, dim3(S_ / 8), dim3(512), 0, stream, (const char*)ws, (unsigned short*)d_out);
}
```

```cpp
#if defined(__HIP_DEVICE_COMPILE__) && __HIP_DEVICE_COMPILE__
#pragma clang attribute push(__attribute__((target("no-packed-fp32-ops"))), apply_to = function)
#define POP_TARGET_ATTR 1
#endif
#define ROT1 1
#define ROT2 0
#define PFWD 4
#include <hip/hip_runtime.h>
#include <hip/hip_bf16.h>
#include <stdint.h>
#define TID_X ((int)__builtin_amdgcn_workitem_id_x())
#define BID_X ((int)__builtin_amdgcn_workgroup_id_x())

typedef short bf16x8 __attribute__((ext_vector_type(8)));
typedef short s16x4 __attribute__((ext_vector_type(4)));
typedef float f32x2 __attribute__((ext_vector_type(2)));
typedef float f32x4 __attribute__((ext_vector_type(4)));
typedef float f32x16 __attribute__((ext_vector_type(16)));
typedef unsigned u32x4 __attribute__((ext_vector_type(4)));
typedef unsigned u32x2 __attribute__((ext_vector_type(2)));
typedef __bf16 bf16x2_t __attribute__((ext_vector_type(2)));
typedef __attribute__((address_space(3))) s16x4* lds_s16x4_ptr;
typedef __attribute__((address_space(3))) void* lds_void_ptr;
typedef const __attribute__((address_space(1))) void* glb_cvoid_ptr;

#define DI __device__ __forceinline__
#define SB() __builtin_amdgcn_sched_barrier(0)

constexpr int S_ = 2048, D_ = 512, F_ = 1376, FP_ = 1408, DF_ = 257;
constexpr int XS_ = 2080;

constexpr size_t WS_XT = 0;
constexpr size_t WS_H = 5u << 20;
constexpr size_t WS_B = 11u << 20;
constexpr size_t WS_WT = 13u << 20;
constexpr size_t WS_G = 16u << 20;
constexpr size_t WS_YT = 17u << 20;

#define TRACE_BEGIN
#define TRACE_END(ws, k)

struct cf { float x, y; };
DI cf mk(float x, float y) { cf r; r.x = x; r.y = y; return r; }

DI cf tw_get(int j) {
  const float r = (float)(j & 2047) * (1.f / 2048.f);
  return mk(__builtin_amdgcn_cosf(r), -__builtin_amdgcn_sinf(r));
}
DI unsigned pk_bf16(float a, float b) {
  f32x2 v = {a, b};
  return __builtin_bit_cast(unsigned, __builtin_convertvector(v, bf16x2_t));
}
DI float lo_f(unsigned w) { return __builtin_bit_cast(float, w << 16); }
DI float hi_f(unsigned w) { return __builtin_bit_cast(float, w & 0xffff0000u); }
DI float rbf(float f) { return lo_f(pk_bf16(f, f)); }
DI float sigm(float z) { return __builtin_amdgcn_rcpf(1.f + __builtin_amdgcn_exp2f(-1.4426950408889634f * z)); }

DI cf cadd(cf a, cf b) { return mk(a.x + b.x, a.y + b.y); }
DI cf csub(cf a, cf b) { return mk(a.x - b.x, a.y - b.y); }
DI cf cmul(cf a, cf b) { return mk(a.x * b.x - a.y * b.y, a.x * b.y + a.y * b.x); }
DI cf cmulc(cf a, cf b) { return mk(a.x * b.x + a.y * b.y, a.y * b.x - a.x * b.y); }
template <int SGN> DI cf mul_i(cf a) { return SGN < 0 ? mk(a.y, -a.x) : mk(-a.y, a.x); }

template <int SGN> DI void dft4(cf& b0, cf& b1, cf& b2, cf& b3) {
  cf c0 = cadd(b0, b2), c1 = csub(b0, b2), c2 = cadd(b1, b3), c3 = mul_i<SGN>(csub(b1, b3));
  b0 = cadd(c0, c2); b2 = csub(c0, c2); b1 = cadd(c1, c3); b3 = csub(c1, c3);
}
template <int SGN> DI void dft8(cf* v) {
  const float r = 0.70710678118654752440f;
  cf a0 = cadd(v[0], v[4]), a4 = csub(v[0], v[4]);
  cf a1 = cadd(v[1], v[5]), a5 = csub(v[1], v[5]);
  cf a2 = cadd(v[2], v[6]), a6 = csub(v[2], v[6]);
  cf a3 = cadd(v[3], v[7]), a7 = csub(v[3], v[7]);
  if (SGN < 0) {
    a5 = mk((a5.x + a5.y) * r, (a5.y - a5.x) * r);
    a7 = mk((a7.y - a7.x) * r, -(a7.x + a7.y) * r);
  } else {
    a5 = mk((a5.x - a5.y) * r, (a5.y + a5.x) * r);
    a7 = mk(-(a7.x + a7.y) * r, (a7.x - a7.y) * r);
  }
  a6 = mul_i<SGN>(a6);
  dft4<SGN>(a0, a1, a2, a3);
  dft4<SGN>(a4, a5, a6, a7);
  v[0] = a0; v[2] = a1; v[4] = a2; v[6] = a3;
  v[1] = a4; v[3] = a5; v[5] = a6; v[7] = a7;
}

DI void wave_sync() {
  __builtin_amdgcn_fence(__ATOMIC_RELEASE, "wavefront");
  __builtin_amdgcn_wave_barrier();
  __builtin_amdgcn_fence(__ATOMIC_ACQUIRE, "wavefront");
}

constexpr int RGN = 292;

DI void fft256_fwd(cf* v, cf* sc, int j, const cf* tw1, const cf* tw2) {
  dft8<-1>(v);
#pragma unroll
  for (int k = 1; k < 8; ++k) v[k] = cmul(v[k], tw1[k - 1]);
#pragma unroll
  for (int k = 0; k < 8; ++k) sc[k * 36 + j] = v[k];
  wave_sync();
  const int k1 = j >> 2, a = j & 3;
#pragma unroll
  for (int m = 0; m < 8; ++m) v[m] = sc[k1 * 36 + 4 * m + a];
  wave_sync();
  dft8<-1>(v);
#pragma unroll
  for (int k = 1; k < 8; ++k) v[k] = cmul(v[k], tw2[k - 1]);
#pragma unroll
  for (int k = 0; k < 8; ++k) sc[k * 33 + j] = v[k];
  wave_sync();
#pragma unroll
  for (int e = 0; e < 2; ++e)
#pragma unroll
    for (int m = 0; m < 4; ++m) v[e * 4 + m] = sc[(a + 4 * e) * 33 + k1 * 4 + m];
  wave_sync();
  dft4<-1>(v[0], v[1], v[2], v[3]);
  dft4<-1>(v[4], v[5], v[6], v[7]);
}
DI void fft256_inv(cf* v, cf* sc, int j, const cf* tw1, const cf* tw2) {
  const int k1 = j >> 2, a = j & 3;
  dft4<1>(v[0], v[1], v[2], v[3]);
  dft4<1>(v[4], v[5], v[6], v[7]);
#pragma unroll
  for (int e = 0; e < 2; ++e)
#pragma unroll
    for (int m = 0; m < 4; ++m) sc[(a + 4 * e) * 33 + k1 * 4 + m] = v[e * 4 + m];
  wave_sync();
#pragma unroll
  for (int k = 0; k < 8; ++k) v[k] = sc[k * 33 + j];
  wave_sync();
#pragma unroll
  for (int k = 1; k < 8; ++k) v[k] = cmulc(v[k], tw2[k - 1]);
  dft8<1>(v);
#pragma unroll
  for (int m = 0; m < 8; ++m) sc[k1 * 36 + 4 * m + a] = v[m];
  wave_sync();
#pragma unroll
  for (int k = 0; k < 8; ++k) v[k] = sc[k * 36 + j];
  wave_sync();
#pragma unroll
  for (int k = 1; k < 8; ++k) v[k] = cmulc(v[k], tw1[k - 1]);
  dft8<1>(v);
}
DI void load_tw256(int j, cf* tw1, cf* tw2) {
#pragma unroll
  for (int k = 1; k < 8; ++k) tw1[k - 1] = tw_get(8 * j * k);
#pragma unroll
  for (int k = 1; k < 8; ++k) tw2[k - 1] = tw_get(64 * (j & 3) * k);
}

#define SM_H 0
#define SM_X 0
#define SM_Y 0
#define SM_B 0
template <int MODE> DI void st16(void* p, u32x4 v) {
  if (MODE == 0) *reinterpret_cast<u32x4*>(p) = v;
  else asm volatile("global_store_dwordx4 %0, %1, off sc1\n\ts_nop 1" ::"v"(p), "v"(v) : "memory");
}
template <int MODE> DI void st8(void* p, u32x2 v) {
  if (MODE == 0) *reinterpret_cast<u32x2*>(p) = v;
  else asm volatile("global_store_dwordx2 %0, %1, off sc1" ::"v"(p), "v"(v) : "memory");
}
DI u32x4 f4u(f32x4 v) { return __builtin_bit_cast(u32x4, v); }

DI void glds16(const void* g, char* lds) {
  __builtin_amdgcn_global_load_lds((glb_cvoid_ptr)g, (lds_void_ptr)lds, 16, 0, 0);
}

constexpr int G1_TILES = 240;
constexpr int AUX_WG = 256;
constexpr int K1_GRID = G1_TILES + AUX_WG;
constexpr int K1_LDS = 81920;

DI void gemm1_role(char* smem, int b, const unsigned short* __restrict__ x, const unsigned short* __restrict__ wg,
                   const unsigned short* __restrict__ wu, unsigned short* __restrict__ H) {
  const int tid = TID_X, lane = tid & 63, wid = tid >> 6;
  const int xcd = b & 7, li = b >> 3;
  const int ft = li >> 1, mt = xcd * 2 + (li & 1);
  const int m0 = mt * 128, f0 = ft * 96;
  char* const As = smem;
  char* const Bs = smem + 32768;

  const unsigned short* asrc[2];
#pragma unroll
  for (int i = 0; i < 2; ++i) {
    const int row = (wid * 2 + i) * 16 + (lane >> 2), p = lane & 3;
    asrc[i] = x + (size_t)(m0 + row) * D_ + (p ^ ((4 - ((row >> 2) & 3)) & 3)) * 8;
  }
  const unsigned short* bsrc[3];
#pragma unroll
  for (int i = 0; i < 3; ++i) {
    const int n16 = 4 * i + (lane >> 4);
    const int rp = (lane >> 1) & 7, hf = lane & 1;
    const int k = wid * 8 + (rp ^ (4 * (wid & 1)));
    int col = f0 + (n16 % 6) * 16 + hf * 8;
    col = col > F_ - 8 ? F_ - 8 : col;
    bsrc[i] = (n16 < 6 ? wg : wu) + (size_t)k * F_ + col;
  }
  const int rot = ROT1 ? __builtin_amdgcn_readfirstlane(2 * ((xcd + ft) & 7)) : 0;
  auto glA = [&](int tt, int i) { glds16(asrc[i] + ((tt + rot) & 15) * 32, As + (tt & 3) * 8192 + (wid * 2 + i) * 1024); };
  auto glB = [&](int tt, int i) { glds16(bsrc[i] + (size_t)((tt + rot) & 15) * 32 * F_, Bs + (tt & 3) * 12288 + (wid * 3 + i) * 1024); };

  const int wm = wid >> 1, wn = wid & 1;
  const int g = lane >> 4, q = (lane & 15) >> 2, p = lane & 3;
  int aoff[4];
#pragma unroll
  for (int i = 0; i < 4; ++i) {
    const int row = wm * 64 + i * 16 + (lane & 15);
    aoff[i] = row * 64 + ((g ^ ((4 - ((row >> 2) & 3)) & 3)) << 4);
  }
  const int bo0 = (g * 12 + wn * 3) * 256 + ((q ^ (4 * (g & 1))) * 32) + p * 8;
  const int bo1 = (g * 12 + wn * 3) * 256 + (((4 + q) ^ (4 * (g & 1))) * 32) + p * 8;

  f32x4 acc[4][6];
#pragma unroll
  for (int i = 0; i < 4; ++i)
#pragma unroll
    for (int j = 0; j < 6; ++j) acc[i][j] = (f32x4){0.f, 0.f, 0.f, 0.f};

  bf16x8 af[2][4];
  s16x4 bl[2][6], bh[2][6];
  auto dsr = [&](int t, int set, int n) {
    const char* ab = As + (t & 3) * 8192;
    const char* bb = Bs + (t & 3) * 12288;
    if (n < 12) {
      const int j = n >> 1, o = (j < 3 ? j : j + 3) * 256;
      if (n & 1) bh[set][j] = __builtin_amdgcn_ds_read_tr16_b64_v4i16((lds_s16x4_ptr)(bb + bo1 + o));
      else bl[set][j] = __builtin_amdgcn_ds_read_tr16_b64_v4i16((lds_s16x4_ptr)(bb + bo0 + o));
    } else {
      af[set][n - 12] = *reinterpret_cast<const bf16x8*>(ab + aoff[n - 12]);
    }
  };
  auto gl = [&](int t, int gi) {
    if (t & 1) {
      if (gi < 4) { glA(t + 3 + (gi & 1), gi >> 1); return; }
      gi -= 4;
    }
    if (gi < 3) glB(t + 4, gi);
  };
  auto mm = [&](int set, int i) {
    const int mi = i / 6, j = i % 6;
    acc[mi][j] = __builtin_amdgcn_mfma_f32_16x16x32_bf16(__builtin_shufflevector(bl[set][j], bh[set][j], 0, 1, 2, 3, 4, 5, 6, 7),
                                                         af[set][mi], acc[mi][j], 0, 0, 0);
  };

  glA(0, 0); glA(1, 0); glA(0, 1); glA(1, 1);
  glB(0, 0); glB(0, 1); glB(0, 2); glB(1, 0); glB(1, 1); glB(1, 2);
  glA(2, 0); glA(3, 0); glA(2, 1); glA(3, 1);
  glB(2, 0); glB(2, 1); glB(2, 2); glB(3, 0); glB(3, 1); glB(3, 2);
  asm volatile("s_waitcnt vmcnt(13)\n\ts_barrier" ::: "memory");
#pragma unroll
  for (int n = 0; n < 16; ++n) dsr(0, 0, n);
  SB();
#pragma unroll
  for (int t = 0; t < 16; ++t) {
    if (t < 15) {
      if (t == 14) asm volatile("s_waitcnt vmcnt(0) lgkmcnt(0)\n\ts_barrier" ::: "memory");
      else if (t == 13) asm volatile("s_waitcnt vmcnt(3) lgkmcnt(0)\n\ts_barrier" ::: "memory");
      else if (t & 1) asm volatile("s_waitcnt vmcnt(6) lgkmcnt(0)\n\ts_barrier" ::: "memory");
      else asm volatile("s_waitcnt vmcnt(10) lgkmcnt(0)\n\ts_barrier" ::: "memory");
    }
    SB();
#pragma unroll
    for (int i = 0; i < 24; ++i) {
      mm(t & 1, i);
      SB();
      if (t < 15 && i < 16) { dsr(t + 1, (t + 1) & 1, i); SB(); }
      if (t + 4 < 16 && (i % 3) == 2) {
        const int gi = i / 3;
        if (gi < ((t & 1) ? 7 : 3)) { gl(t, gi); SB(); }
      }
    }
  }
  asm volatile("s_waitcnt vmcnt(0) lgkmcnt(0)\n\ts_barrier" ::: "memory");

  constexpr int HS = 56;
  unsigned short* tl = reinterpret_cast<unsigned short*>(smem) + wid * (64 * HS);
#pragma unroll
  for (int hf = 0; hf < 2; ++hf) {
#pragma unroll
    for (int m2 = 0; m2 < 2; ++m2)
#pragma unroll
      for (int j = 0; j < 3; ++j) {
        const int mi = hf * 2 + m2;
        u32x2 o;
#pragma unroll
        for (int e = 0; e < 2; ++e) {
          const unsigned gp = pk_bf16(acc[mi][j][2 * e], acc[mi][j][2 * e + 1]);
          const unsigned up = pk_bf16(acc[mi][j + 3][2 * e], acc[mi][j + 3][2 * e + 1]);
          const float g0 = lo_f(gp), g1 = hi_f(gp);
          const unsigned sp = pk_bf16(g0 * sigm(g0), g1 * sigm(g1));
          o[e] = pk_bf16(lo_f(sp) * lo_f(up), hi_f(sp) * hi_f(up));
        }
        *reinterpret_cast<u32x2*>(tl + (mi * 16 + (lane & 15)) * HS + j * 16 + g * 4) = o;
      }
    wave_sync();
#pragma unroll
    for (int i = 0; i < 3; ++i) {
      const int c = lane + 64 * i, row = hf * 32 + c / 6, ch = c % 6;
      const int f = f0 + wn * 48 + ch * 8;
      if (f < FP_) {
        u32x4 v = *reinterpret_cast<const u32x4*>(tl + row * HS + ch * 8);
        if (f >= F_) v = (u32x4){0u, 0u, 0u, 0u};
        st16<SM_H>(H + (size_t)(m0 + wm * 64 + row) * FP_ + f, v);
      }
    }
    SB();
  }
}

DI void aux1_role(char* smem, int b, const unsigned short* __restrict__ x, const unsigned short* __restrict__ gw,
                  const unsigned short* __restrict__ wr, const unsigned short* __restrict__ wi, cf* __restrict__ XT,
                  float* __restrict__ G, unsigned* __restrict__ WT, const unsigned short* __restrict__ wd,
                  const unsigned short* __restrict__ wg, const unsigned short* __restrict__ wu) {
  const int tid = TID_X, j = tid & 31, hw = tid >> 5;
  unsigned pf = 0;
  if (PFWD && tid < 192) {
    const int m = tid >> 6, l = tid & 63;
    if (l < 43 && ((PFWD >> m) & 1)) {
      const unsigned short* wsel = m == 0 ? wg : (m == 1 ? wu : wd);
      pf = reinterpret_cast<const unsigned*>(wsel)[((size_t)b * 43 + l) * 32];
    }
  }
  const int s0 = b * 8, s = s0 + hw;
  cf* sc = reinterpret_cast<cf*>(smem) + hw * RGN;
  const unsigned* xr = reinterpret_cast<const unsigned*>(x + (size_t)s * D_);
  const unsigned* gr = reinterpret_cast<const unsigned*>(gw);
  unsigned xv[8], gv[8];
#pragma unroll
  for (int r = 0; r < 8; ++r) xv[r] = xr[j + 32 * r];
#pragma unroll
  for (int r = 0; r < 8; ++r) gv[r] = gr[j + 32 * r];
  const u32x4* sr4 = reinterpret_cast<const u32x4*>(wr + (size_t)s0 * DF_);
  const u32x4* si4 = reinterpret_cast<const u32x4*>(wi + (size_t)s0 * DF_);
  const u32x4 w0 = sr4[tid], w1 = si4[tid];
  u32x4 w2 = w0;
  if (tid < 2) w2 = (tid == 0 ? sr4 : si4)[256];
  cf tw1[7], tw2[7];
  load_tw256(j, tw1, tw2);
  u32x4* lw = reinterpret_cast<u32x4*>(smem + 20480);
  lw[tid] = w0; lw[258 + tid] = w1;
  if (tid < 2) lw[tid * 258 + 256] = w2;

  cf v[8];
  float gs = 0.f;
#pragma unroll
  for (int r = 0; r < 8; ++r) {
    v[r] = mk(lo_f(xv[r]), hi_f(xv[r]));
    const unsigned pp = pk_bf16(v[r].x * lo_f(gv[r]), v[r].y * hi_f(gv[r]));
    gs += lo_f(pp);
    gs += hi_f(pp);
  }
#pragma unroll
  for (int o = 16; o > 0; o >>= 1) gs += __shfl_xor(gs, o);
  if (j == 0) G[s] = rbf(sigm(rbf(gs)));
  fft256_fwd(v, sc, j, tw1, tw2);
  {
    const int k1 = j >> 2, a = j & 3;
#pragma unroll
    for (int e = 0; e < 2; ++e)
#pragma unroll
      for (int l = 0; l < 4; ++l) sc[k1 + 8 * (a + 4 * e) + 64 * l] = v[e * 4 + l];
  }
  __syncthreads();
  {
    const int sp = (tid & 3) * 2, kk = tid >> 2;
    const cf* z0 = reinterpret_cast<const cf*>(smem) + sp * RGN;
    const cf* z1 = z0 + RGN;
#pragma unroll
    for (int i = 0; i < 2; ++i) {
      const int k = kk + 64 * i;
      if (k == 0) {
        const cf a0 = z0[0], ah = z0[128], b0 = z1[0], bh = z1[128];
        st16<SM_X>(XT + s0 + sp, f4u((f32x4){a0.x + a0.y, a0.x - a0.y, b0.x + b0.y, b0.x - b0.y}));
        st16<SM_X>(XT + (size_t)128 * XS_ + s0 + sp, f4u((f32x4){ah.x, -ah.y, bh.x, -bh.y}));
      } else {
        const cf w = tw_get(4 * k);
        f32x4 o0, o1;
#pragma unroll
        for (int rr = 0; rr < 2; ++rr) {
          const cf za = (rr ? z1 : z0)[k], zb = (rr ? z1 : z0)[256 - k];
          const cf E = mk(0.5f * (za.x + zb.x), 0.5f * (za.y - zb.y));
          const cf O = mk(0.5f * (za.y + zb.y), 0.5f * (zb.x - za.x));
          const cf wo = cmul(O, w);
          o0[2 * rr] = E.x + wo.x; o0[2 * rr + 1] = E.y + wo.y;
          o1[2 * rr] = E.x - wo.x; o1[2 * rr + 1] = wo.y - E.y;
        }
        st16<SM_X>(XT + (size_t)k * XS_ + s0 + sp, f4u(o0));
        st16<SM_X>(XT + (size_t)(256 - k) * XS_ + s0 + sp, f4u(o1));
      }
    }
  }
  {
    const unsigned short* fr = reinterpret_cast<const unsigned short*>(lw);
    const unsigned short* fi = reinterpret_cast<const unsigned short*>(lw + 258);
    const int r4 = (tid & 1) * 4;
#pragma unroll
    for (int it = 0; it < 3; ++it) {
      const int c = (tid >> 1) + 128 * it;
      if (c < DF_) {
        u32x4 o;
#pragma unroll
        for (int e = 0; e < 4; ++e) o[e] = (unsigned)fr[(r4 + e) * DF_ + c] | ((unsigned)fi[(r4 + e) * DF_ + c] << 16);
        st16<SM_X>(WT + (size_t)c * XS_ + s0 + r4, o);
      }
    }
  }
  asm volatile("" ::"v"(pf));
}

__global__ __launch_bounds__(256, 2) void k1_kernel(const unsigned short* __restrict__ x, const unsigned short* __restrict__ wr,
                                                    const unsigned short* __restrict__ wi, const unsigned short* __restrict__ wg,
                                                    const unsigned short* __restrict__ wu, const unsigned short* __restrict__ gw,
                                                    const unsigned short* __restrict__ wd, char* __restrict__ ws) {
  extern __shared__ __attribute__((aligned(16))) char smem[];
  const int b = BID_X;
  TRACE_BEGIN
  const int bg = b, ba = b - G1_TILES;
  if (b < G1_TILES) {
    gemm1_role(smem, bg, x, wg, wu, reinterpret_cast<unsigned short*>(ws + WS_H));
  } else {
    aux1_role(smem, (ba & 7) * 32 + (ba >> 3), x, gw, wr, wi, reinterpret_cast<cf*>(ws + WS_XT), reinterpret_cast<float*>(ws + WS_G),
              reinterpret_cast<unsigned*>(ws + WS_WT), wd, wg, wu);
  }
  TRACE_END(ws, 0)
}

constexpr int G2_TILES = 256;
constexpr int K2_GRID = G2_TILES + 256;
constexpr int K2_LDS = 81920;

DI void gemm2_role(char* smem, int b, const unsigned short* __restrict__ H, const unsigned short* __restrict__ wd,
                   unsigned short* __restrict__ Bo) {
  const int tid = TID_X, lane = tid & 63, wid = tid >> 6;
  const int xcd = b & 7, li = b >> 3;
  const int mt = xcd * 4 + (li >> 3), nt = li & 7;
  const int m0 = mt * 64, n0 = nt * 64;
  const unsigned short* asrc[2];
  int bk[2];
#pragma unroll
  for (int i = 0; i < 2; ++i) {
    const int row = (wid * 2 + i) * 8 + (lane >> 3), c = (lane & 7) ^ ((row >> 1) & 7);
    asrc[i] = H + (size_t)(m0 + row) * FP_ + c * 8;
    bk[i] = (wid * 2 + i) * 8 + (((lane >> 1) & 7) ^ (4 * ((lane >> 4) & 1)));
  }
  const unsigned short* bsrc = wd + n0 + (lane >> 4) * 16 + (lane & 1) * 8;
  const int rot = ROT2 ? __builtin_amdgcn_readfirstlane((mt * 11) % 22 + (mt >> 1) % 11) % 22 : 0;
  auto gl = [&](int t, int i) {
    char* buf = smem + (t % 5) * 16384;
    int kt = t + rot;
    kt = kt >= 22 ? kt - 22 : kt;
    if (i < 2) glds16(asrc[i] + kt * 64, buf + (wid * 2 + i) * 1024);
    else {
      int k = kt * 64 + bk[i - 2];
      k = k < F_ ? k : F_ - 1;
      glds16(bsrc + (size_t)k * D_, buf + 8192 + (wid * 2 + i - 2) * 1024);
    }
  };
  const int wm = wid >> 1, wn = wid & 1;
  const int h = lane >> 5, gi = lane >> 4, q = (lane & 15) >> 2, p = lane & 3;
  const int arow = wm * 32 + (lane & 31);
  int aoff[4];
#pragma unroll
  for (int ks = 0; ks < 4; ++ks) aoff[ks] = arow * 128 + (((2 * ks + h) ^ ((arow >> 1) & 7)) << 4);
  const int bbase = 8192 + (h * 4 + wn * 2 + (gi & 1)) * 256 + p * 8;
  const int bo0 = bbase + ((q ^ (4 * (gi & 1))) * 32), bo1 = bbase + (((4 + q) ^ (4 * (gi & 1))) * 32);
  f32x16 acc, acc2;
#pragma unroll
  for (int r = 0; r < 16; ++r) { acc[r] = 0.f; acc2[r] = 0.f; }
  bf16x8 a[2][4];
  s16x4 bl[2][4], bh[2][4];
  auto dsr = [&](int t, int set, int ks) {
    const char* buf = smem + (t % 5) * 16384;
    a[set][ks] = *reinterpret_cast<const bf16x8*>(buf + aoff[ks]);
    bl[set][ks] = __builtin_amdgcn_ds_read_tr16_b64_v4i16((lds_s16x4_ptr)(buf + bo0 + ks * 2048));
    bh[set][ks] = __builtin_amdgcn_ds_read_tr16_b64_v4i16((lds_s16x4_ptr)(buf + bo1 + ks * 2048));
  };
  constexpr int NT = 22;
#pragma unroll
  for (int t = 0; t < 5; ++t)
#pragma unroll
    for (int i = 0; i < 4; ++i) gl(t, i);
#define PRIOG2L 1
  __builtin_amdgcn_s_setprio(PRIOG2L);
  asm volatile("s_waitcnt vmcnt(16)\n\ts_barrier" ::: "memory");
#pragma unroll
  for (int i = 0; i < 4; ++i) dsr(0, 0, i);
  SB();
#pragma unroll
  for (int t = 0; t < NT; ++t) {
    if (t < NT - 1) {
      const int later = (t + 4 < NT ? t + 4 : NT - 1) - (t + 1);
      if (later == 3) asm volatile("s_waitcnt vmcnt(12) lgkmcnt(0)\n\ts_barrier" ::: "memory");
      else if (later == 2) asm volatile("s_waitcnt vmcnt(8) lgkmcnt(0)\n\ts_barrier" ::: "memory");
      else if (later == 1) asm volatile("s_waitcnt vmcnt(4) lgkmcnt(0)\n\ts_barrier" ::: "memory");
      else asm volatile("s_waitcnt vmcnt(0) lgkmcnt(0)\n\ts_barrier" ::: "memory");
    }
    SB();
#pragma unroll
    for (int i = 0; i < 4; ++i) {
      if (i & 1) acc2 = __builtin_amdgcn_mfma_f32_32x32x16_bf16(__builtin_shufflevector(bl[t & 1][i], bh[t & 1][i], 0, 1, 2, 3, 4, 5, 6, 7),
                                                                a[t & 1][i], acc2, 0, 0, 0);
      else acc = __builtin_amdgcn_mfma_f32_32x32x16_bf16(__builtin_shufflevector(bl[t & 1][i], bh[t & 1][i], 0, 1, 2, 3, 4, 5, 6, 7),
                                                         a[t & 1][i], acc, 0, 0, 0);
      SB();
      if (t < NT - 1) { dsr(t + 1, (t + 1) & 1, i); SB(); }
      if (t + 5 < NT) { gl(t + 5, i); SB(); }
    }
  }
#pragma unroll
  for (int r = 0; r < 16; ++r) acc[r] += acc2[r];
  unsigned short* drow = Bo + (size_t)(m0 + arow) * D_ + n0 + wn * 32 + h * 8;
#pragma unroll
  for (int rp = 0; rp < 2; ++rp) {
    unsigned e0 = pk_bf16(acc[8 * rp], acc[8 * rp + 1]), e1 = pk_bf16(acc[8 * rp + 2], acc[8 * rp + 3]);
    unsigned o0 = pk_bf16(acc[8 * rp + 4], acc[8 * rp + 5]), o1 = pk_bf16(acc[8 * rp + 6], acc[8 * rp + 7]);
    auto r0 = __builtin_amdgcn_permlane32_swap(e0, o0, false, false);
    auto r1 = __builtin_amdgcn_permlane32_swap(e1, o1, false, false);
    st16<SM_B>(drow + rp * 16, (u32x4){r0[0], r1[0], r0[1], r1[1]});
  }
}

DI int col_u(int t, int slot) {
  const int j = t & 31;
  return (t >> 5) + 8 * ((j >> 2) + 8 * ((j & 3) + 4 * (slot >> 2)) + 64 * (slot & 3));
}

DI void fft2_role(char* smem, int col, const cf* __restrict__ XT, const unsigned* __restrict__ WT, cf* __restrict__ YT) {
  const int t = TID_X, j = t & 31, hw = t >> 5;
  cf* all = reinterpret_cast<cf*>(smem);
  cf* sc = all + hw * RGN;
  const cf* xc = XT + (size_t)col * XS_;
  cf v[8];
#pragma unroll
  for (int r = 0; r < 8; ++r) v[r] = xc[t + 256 * r];
  unsigned w[8], wm[8], wn[8], wnm[8];
#pragma unroll
  for (int r = 0; r < 8; ++r) w[r] = WT[(size_t)col * XS_ + col_u(t, r)];
  if (col == 0) {
#pragma unroll
    for (int r = 0; r < 8; ++r) {
      const int u = col_u(t, r), um = (S_ - u) & (S_ - 1);
      wm[r] = WT[um]; wn[r] = WT[(size_t)256 * XS_ + u]; wnm[r] = WT[(size_t)256 * XS_ + um];
    }
  }
  cf tw0[7], tw1[7], tw2[7];
#pragma unroll
  for (int k = 1; k < 8; ++k) tw0[k - 1] = tw_get(t * k);
  load_tw256(j, tw1, tw2);

  dft8<-1>(v);
#pragma unroll
  for (int k = 1; k < 8; ++k) v[k] = cmul(v[k], tw0[k - 1]);
#pragma unroll
  for (int k = 0; k < 8; ++k) all[k * RGN + t] = v[k];
  __syncthreads();
#pragma unroll
  for (int r = 0; r < 8; ++r) v[r] = sc[j + 32 * r];
  wave_sync();
  fft256_fwd(v, sc, j, tw1, tw2);

  if (col != 0) {
#pragma unroll
    for (int r = 0; r < 8; ++r) v[r] = cmul(v[r], mk(lo_f(w[r]), hi_f(w[r])));
  } else {
    cf* nat = all + 8 * RGN;
#pragma unroll
    for (int r = 0; r < 8; ++r) { const int u = col_u(t, r); nat[(u & 7) * 260 + (u >> 3)] = v[r]; }
    __syncthreads();
#pragma unroll
    for (int r = 0; r < 8; ++r) {
      const int um = (S_ - col_u(t, r)) & (S_ - 1);
      const cf P = v[r], M = nat[(um & 7) * 260 + (um >> 3)];
      const cf X0 = mk(0.5f * (P.x + M.x), 0.5f * (P.y - M.y));
      const cf XN = mk(0.5f * (P.y + M.y), 0.5f * (M.x - P.x));
      const cf W0 = mk(0.5f * (lo_f(w[r]) + lo_f(wm[r])), 0.5f * (hi_f(w[r]) - hi_f(wm[r])));
      const cf WN = mk(0.5f * (lo_f(wn[r]) + lo_f(wnm[r])), 0.5f * (hi_f(wn[r]) - hi_f(wnm[r])));
      const cf Z0 = cmul(X0, W0), ZN = cmul(XN, WN);
      v[r] = mk(Z0.x - ZN.y, Z0.y + ZN.x);
    }
  }

  fft256_inv(v, sc, j, tw1, tw2);
#pragma unroll
  for (int r = 0; r < 8; ++r) sc[j + 32 * r] = v[r];
  __syncthreads();
#pragma unroll
  for (int k = 0; k < 8; ++k) v[k] = all[k * RGN + t];
#pragma unroll
  for (int k = 1; k < 8; ++k) v[k] = cmulc(v[k], tw0[k - 1]);
  dft8<1>(v);
#pragma unroll
  for (int r = 0; r < 8; ++r)
    st8<SM_Y>(YT + ((size_t)((t >> 3) + 32 * r) * 256 + col) * 8 + (t & 7), (u32x2){__builtin_bit_cast(unsigned, v[r].x), __builtin_bit_cast(unsigned, v[r].y)});
}

__global__ __launch_bounds__(256, 2) void k2_kernel(const unsigned short* __restrict__ wd, char* __restrict__ ws) {
  extern __shared__ __attribute__((aligned(16))) char smem[];
  const int b = BID_X;
  TRACE_BEGIN
  if (b >= 256) {
    gemm2_role(smem, b - 256, reinterpret_cast<const unsigned short*>(ws + WS_H), wd, reinterpret_cast<unsigned short*>(ws + WS_B));
  } else {
    fft2_role(smem, (b & 7) * 32 + (b >> 3), reinterpret_cast<const cf*>(ws + WS_XT), reinterpret_cast<const unsigned*>(ws + WS_WT),
              reinterpret_cast<cf*>(ws + WS_YT));
  }
  TRACE_END(ws, 1)
}

constexpr int R3 = 322;
__global__ __launch_bounds__(512) void k3_kernel(const char* __restrict__ ws, unsigned short* __restrict__ out) {
  __shared__ __attribute__((aligned(16))) cf smem[8 * R3];
  TRACE_BEGIN
  const int tid = TID_X, lane = tid & 63, wv = tid >> 6;
  const int blk = (BID_X & 7) * 32 + (BID_X >> 3);
  const int s0 = blk * 8, s = s0 + wv;
  const cf* YT = reinterpret_cast<const cf*>(ws + WS_YT) + (size_t)blk * 2048;
  const unsigned* Bo = reinterpret_cast<const unsigned*>(ws + WS_B) + (size_t)s * (D_ / 2);
  const float* G = reinterpret_cast<const float*>(ws + WS_G);
  const int sr = tid & 7, kk = tid >> 3;
  cf ya[2], yb[2], twk[2];
#pragma unroll
  for (int i = 0; i < 2; ++i) {
    const int k = kk + 64 * i;
    ya[i] = YT[k * 8 + sr];
    yb[i] = YT[(k == 0 ? 128 : 256 - k) * 8 + sr];
  }
  unsigned bv[4];
#pragma unroll
  for (int r = 0; r < 4; ++r) bv[r] = Bo[lane + 64 * r];
  const float g = G[s];
  cf t1[3], t2[3], t3[3];
#pragma unroll
  for (int i = 0; i < 2; ++i) twk[i] = tw_get(4 * (kk + 64 * i));
#pragma unroll
  for (int k = 1; k < 4; ++k) {
    t1[k - 1] = tw_get(8 * lane * k);
    t2[k - 1] = tw_get(32 * (lane & 15) * k);
    t3[k - 1] = tw_get(128 * (lane & 3) * k);
  }
  cf* zr = smem + sr * R3;
#pragma unroll
  for (int i = 0; i < 2; ++i) {
    const int k = kk + 64 * i;
    if (k == 0) {
      zr[0] = mk(ya[i].x + ya[i].y, ya[i].x - ya[i].y);
      zr[128] = mk(2.f * yb[i].x, -2.f * yb[i].y);
    } else {
      const cf A = mk(ya[i].x + yb[i].x, ya[i].y - yb[i].y);
      const cf Bv = mk(ya[i].x - yb[i].x, ya[i].y + yb[i].y);
      const cf wb = cmulc(Bv, twk[i]);
      const cf T = mk(-wb.y, wb.x);
      zr[k] = cadd(A, T);
      zr[256 - k] = mk(A.x - T.x, T.y - A.y);
    }
  }
  __syncthreads();
  cf* sc = smem + wv * R3;
  cf v[4];
  {
    const int kb = (lane >> 4) + 4 * ((lane >> 2) & 3) + 16 * (lane & 3);
#pragma unroll
    for (int l = 0; l < 4; ++l) v[l] = sc[kb + 64 * l];
  }
  wave_sync();
  dft4<1>(v[0], v[1], v[2], v[3]);
#pragma unroll
  for (int p2 = 0; p2 < 4; ++p2) sc[(lane & 3) * 65 + (lane & ~3) + p2] = v[p2];
  wave_sync();
#pragma unroll
  for (int i1 = 0; i1 < 4; ++i1) v[i1] = sc[i1 * 65 + lane];
  wave_sync();
#pragma unroll
  for (int k = 1; k < 4; ++k) v[k] = cmulc(v[k], t3[k - 1]);
  dft4<1>(v[0], v[1], v[2], v[3]);
#pragma unroll
  for (int p1 = 0; p1 < 4; ++p1) sc[(lane >> 2) * 20 + 4 * p1 + (lane & 3)] = v[p1];
  wave_sync();
#pragma unroll
  for (int j1 = 0; j1 < 4; ++j1) v[j1] = sc[((lane >> 4) * 4 + j1) * 20 + (lane & 15)];
  wave_sync();
#pragma unroll
  for (int k = 1; k < 4; ++k) v[k] = cmulc(v[k], t2[k - 1]);
  dft4<1>(v[0], v[1], v[2], v[3]);
#pragma unroll
  for (int m1 = 0; m1 < 4; ++m1) sc[(lane >> 4) * 80 + 16 * m1 + (lane & 15)] = v[m1];
  wave_sync();
#pragma unroll
  for (int k1 = 0; k1 < 4; ++k1) v[k1] = sc[k1 * 80 + lane];
#pragma unroll
  for (int k = 1; k < 4; ++k) v[k] = cmulc(v[k], t1[k - 1]);
  dft4<1>(v[0], v[1], v[2], v[3]);
  const float sc20 = 1.f / 1048576.f;
  const float og = rbf(1.f - g);
  unsigned* orow = reinterpret_cast<unsigned*>(out + (size_t)s * D_);
#pragma unroll
  for (int r = 0; r < 4; ++r) {
    const unsigned ap = pk_bf16(v[r].x * sc20, v[r].y * sc20);
    const unsigned ga = pk_bf16(g * lo_f(ap), g * hi_f(ap));
    const unsigned gb = pk_bf16(og * lo_f(bv[r]), og * hi_f(bv[r]));
    orow[lane + 64 * r] = pk_bf16(lo_f(ga) + lo_f(gb), hi_f(ga) + hi_f(gb));
  }
  TRACE_END(ws, 2)
}

#ifdef POP_TARGET_ATTR
#pragma clang attribute pop
#endif

extern "C" void kernel_launch(void* const* d_in, const int* in_sizes, int n_in, void* d_out, int out_size, void* d_ws,
                              size_t ws_size, hipStream_t stream) {
  const unsigned short* x = (const unsigned short*)d_in[0];
  const unsigned short* wr = (const unsigned short*)d_in[1];
  const unsigned short* wi = (const unsigned short*)d_in[2];
  const unsigned short* wg = (const unsigned short*)d_in[4];
  const unsigned short* wu = (const unsigned short*)d_in[5];
  const unsigned short* wd = (const unsigned short*)d_in[6];
  const unsigned short* gw = (const unsigned short*)d_in[7];
  char* ws = (char*)d_ws;
  (void)hipFuncSetAttribute((const void*)k1_kernel, hipFuncAttributeMaxDynamicSharedMemorySize, K1_LDS);
  (void)hipFuncSetAttribute((const void*)k2_kernel, hipFuncAttributeMaxDynamicSharedMemorySize, K2_LDS);
  hipLaunchKernelGGL(k1_kernel, dim3(K1_GRID), dim3(256), K1_LDS, stream, x, wr, wi, wg, wu, gw, wd, ws);
  hipLaunchKernelGGL(k2_kernel, dim3(K2_GRID), dim3(256), K2_LDS, stream, wd, ws);
  hipLaunchKernelGGL(k3_kernel, dim3(S_ / 8), dim3(512), 0, stream, (const char*)ws, (unsigned short*)d_out);
}
```

```cpp
#if defined(__HIP_DEVICE_COMPILE__) && __HIP_DEVICE_COMPILE__
#pragma clang attribute push(__attribute__((target("no-packed-fp32-ops"))), apply_to = function)
#define POP_TARGET_ATTR 1
#endif
#include <hip/hip_runtime.h>
#include <hip/hip_bf16.h>
#include <stdint.h>
#define TID_X ((int)__builtin_amdgcn_workitem_id_x())
#define BID_X ((int)__builtin_amdgcn_workgroup_id_x())

typedef short bf16x8 __attribute__((ext_vector_type(8)));
typedef short s16x4 __attribute__((ext_vector_type(4)));
typedef float f32x2 __attribute__((ext_vector_type(2)));
typedef float f32x4 __attribute__((ext_vector_type(4)));
typedef float f32x16 __attribute__((ext_vector_type(16)));
typedef unsigned u32x4 __attribute__((ext_vector_type(4)));
typedef unsigned u32x2 __attribute__((ext_vector_type(2)));
typedef __bf16 bf16x2_t __attribute__((ext_vector_type(2)));
typedef __attribute__((address_space(3))) s16x4* lds_s16x4_ptr;
typedef __attribute__((address_space(3))) void* lds_void_ptr;
typedef const __attribute__((address_space(1))) void* glb_cvoid_ptr;

#define DI __device__ __forceinline__
#define SB() __builtin_amdgcn_sched_barrier(0)

constexpr int S_ = 2048, D_ = 512, F_ = 1376, FP_ = 1408, DF_ = 257;
constexpr int XS_ = 2080;

constexpr size_t WS_XT = 0;
constexpr size_t WS_H = 5u << 20;
constexpr size_t WS_B = 11u << 20;
constexpr size_t WS_WT = 13u << 20;
constexpr size_t WS_G = 16u << 20;
constexpr size_t WS_YT = 17u << 20;

struct cf { float x, y; };
DI cf mk(float x, float y) { cf r; r.x = x; r.y = y; return r; }

DI cf tw_get(int j) {
  const float r = (float)(j & 2047) * (1.f / 2048.f);
  return mk(__builtin_amdgcn_cosf(r), -__builtin_amdgcn_sinf(r));
}
DI unsigned pk_bf16(float a, float b) {
  f32x2 v = {a, b};
  return __builtin_bit_cast(unsigned, __builtin_convertvector(v, bf16x2_t));
}
DI float lo_f(unsigned w) { return __builtin_bit_cast(float, w << 16); }
DI float hi_f(unsigned w) { return __builtin_bit_cast(float, w & 0xffff0000u); }
DI float rbf(float f) { return lo_f(pk_bf16(f, f)); }
DI float sigm(float z) { return __builtin_amdgcn_rcpf(1.f + __builtin_amdgcn_exp2f(-1.4426950408889634f * z)); }

DI cf cadd(cf a, cf b) { return mk(a.x + b.x, a.y + b.y); }
DI cf csub(cf a, cf b) { return mk(a.x - b.x, a.y - b.y); }
DI cf cmul(cf a, cf b) { return mk(a.x * b.x - a.y * b.y, a.x * b.y + a.y * b.x); }
DI cf cmulc(cf a, cf b) { return mk(a.x * b.x + a.y * b.y, a.y * b.x - a.x * b.y); }
template <int SGN> DI cf mul_i(cf a) { return SGN < 0 ? mk(a.y, -a.x) : mk(-a.y, a.x); }

template <int SGN> DI void dft4(cf& b0, cf& b1, cf& b2, cf& b3) {
  cf c0 = cadd(b0, b2), c1 = csub(b0, b2), c2 = cadd(b1, b3), c3 = mul_i<SGN>(csub(b1, b3));
  b0 = cadd(c0, c2); b2 = csub(c0, c2); b1 = cadd(c1, c3); b3 = csub(c1, c3);
}
template <int SGN> DI void dft8(cf* v) {
  const float r = 0.70710678118654752440f;
  cf a0 = cadd(v[0], v[4]), a4 = csub(v[0], v[4]);
  cf a1 = cadd(v[1], v[5]), a5 = csub(v[1], v[5]);
  cf a2 = cadd(v[2], v[6]), a6 = csub(v[2], v[6]);
  cf a3 = cadd(v[3], v[7]), a7 = csub(v[3], v[7]);
  if (SGN < 0) {
    a5 = mk((a5.x + a5.y) * r, (a5.y - a5.x) * r);
    a7 = mk((a7.y - a7.x) * r, -(a7.x + a7.y) * r);
  } else {
    a5 = mk((a5.x - a5.y) * r, (a5.y + a5.x) * r);
    a7 = mk(-(a7.x + a7.y) * r, (a7.x - a7.y) * r);
  }
  a6 = mul_i<SGN>(a6);
  dft4<SGN>(a0, a1, a2, a3);
  dft4<SGN>(a4, a5, a6, a7);
  v[0] = a0; v[2] = a1; v[4] = a2; v[6] = a3;
  v[1] = a4; v[3] = a5; v[5] = a6; v[7] = a7;
}

DI void wave_sync() {
  __builtin_amdgcn_fence(__ATOMIC_RELEASE, "wavefront");
  __builtin_amdgcn_wave_barrier();
  __builtin_amdgcn_fence(__ATOMIC_ACQUIRE, "wavefront");
}

constexpr int RGN = 292;

DI void fft256_fwd(cf* v, cf* sc, int j, const cf* tw1, const cf* tw2) {
  dft8<-1>(v);
#pragma unroll
  for (int k = 1; k < 8; ++k) v[k] = cmul(v[k], tw1[k - 1]);
#pragma unroll
  for (int k = 0; k < 8; ++k) sc[k * 36 + j] = v[k];
  wave_sync();
  const int k1 = j >> 2, a = j & 3;
#pragma unroll
  for (int m = 0; m < 8; ++m) v[m] = sc[k1 * 36 + 4 * m + a];
  wave_sync();
  dft8<-1>(v);
#pragma unroll
  for (int k = 1; k < 8; ++k) v[k] = cmul(v[k], tw2[k - 1]);
#pragma unroll
  for (int k = 0; k < 8; ++k) sc[k * 33 + j] = v[k];
  wave_sync();
#pragma unroll
  for (int e = 0; e < 2; ++e)
#pragma unroll
    for (int m = 0; m < 4; ++m) v[e * 4 + m] = sc[(a + 4 * e) * 33 + k1 * 4 + m];
  wave_sync();
  dft4<-1>(v[0], v[1], v[2], v[3]);
  dft4<-1>(v[4], v[5], v[6], v[7]);
}
DI void fft256_inv(cf* v, cf* sc, int j, const cf* tw1, const cf* tw2) {
  const int k1 = j >> 2, a = j & 3;
  dft4<1>(v[0], v[1], v[2], v[3]);
  dft4<1>(v[4], v[5], v[6], v[7]);
#pragma unroll
  for (int e = 0; e < 2; ++e)
#pragma unroll
    for (int m = 0; m < 4; ++m) sc[(a + 4 * e) * 33 + k1 * 4 + m] = v[e * 4 + m];
  wave_sync();
#pragma unroll
  for (int k = 0; k < 8; ++k) v[k] = sc[k * 33 + j];
  wave_sync();
#pragma unroll
  for (int k = 1; k < 8; ++k) v[k] = cmulc(v[k], tw2[k - 1]);
  dft8<1>(v);
#pragma unroll
  for (int m = 0; m < 8; ++m) sc[k1 * 36 + 4 * m + a] = v[m];
  wave_sync();
#pragma unroll
  for (int k = 0; k < 8; ++k) v[k] = sc[k * 36 + j];
  wave_sync();
#pragma unroll
  for (int k = 1; k < 8; ++k) v[k] = cmulc(v[k], tw1[k - 1]);
  dft8<1>(v);
}
DI void load_tw256(int j, cf* tw1, cf* tw2) {
#pragma unroll
  for (int k = 1; k < 8; ++k) tw1[k - 1] = tw_get(8 * j * k);
#pragma unroll
  for (int k = 1; k < 8; ++k) tw2[k - 1] = tw_get(64 * (j & 3) * k);
}

DI void st16(void* p, u32x4 v) { *reinterpret_cast<u32x4*>(p) = v; }
DI void st8(void* p, u32x2 v) { *reinterpret_cast<u32x2*>(p) = v; }
DI u32x4 f4u(f32x4 v) { return __builtin_bit_cast(u32x4, v); }

DI void glds16(const void* g, char* lds) {
  __builtin_amdgcn_global_load_lds((glb_cvoid_ptr)g, (lds_void_ptr)lds, 16, 0, 0);
}

constexpr int G1_TILES = 240;
constexpr int AUX_WG = 256;
constexpr int K1_GRID = G1_TILES + AUX_WG;
constexpr int K1_LDS = 81920;

DI void gemm1_role(char* smem, int b, const unsigned short* __restrict__ x, const unsigned short* __restrict__ wg,
                   const unsigned short* __restrict__ wu, unsigned short* __restrict__ H) {
  const int tid = TID_X, lane = tid & 63, wid = tid >> 6;
  const int xcd = b & 7, li = b >> 3;
  const int ft = li >> 1, mt = xcd * 2 + (li & 1);
  const int m0 = mt * 128, f0 = ft * 96;
  char* const As = smem;
  char* const Bs = smem + 32768;

  const unsigned short* asrc[2];
#pragma unroll
  for (int i = 0; i < 2; ++i) {
    const int row = (wid * 2 + i) * 16 + (lane >> 2), p = lane & 3;
    asrc[i] = x + (size_t)(m0 + row) * D_ + (p ^ ((4 - ((row >> 2) & 3)) & 3)) * 8;
  }
  const unsigned short* bsrc[3];
#pragma unroll
  for (int i = 0; i < 3; ++i) {
    const int n16 = 4 * i + (lane >> 4);
    const int rp = (lane >> 1) & 7, hf = lane & 1;
    const int k = wid * 8 + (rp ^ (4 * (wid & 1)));
    int col = f0 + (n16 % 6) * 16 + hf * 8;
    col = col > F_ - 8 ? F_ - 8 : col;
    bsrc[i] = (n16 < 6 ? wg : wu) + (size_t)k * F_ + col;
  }
  const int rot = __builtin_amdgcn_readfirstlane(2 * ((xcd + ft) & 7));
  auto glA = [&](int tt, int i) { glds16(asrc[i] + ((tt + rot) & 15) * 32, As + (tt & 3) * 8192 + (wid * 2 + i) * 1024); };
  auto glB = [&](int tt, int i) { glds16(bsrc[i] + (size_t)((tt + rot) & 15) * 32 * F_, Bs + (tt & 3) * 12288 + (wid * 3 + i) * 1024); };

  const int wm = wid >> 1, wn = wid & 1;
  const int g = lane >> 4, q = (lane & 15) >> 2, p = lane & 3;
  int aoff[4];
#pragma unroll
  for (int i = 0; i < 4; ++i) {
    const int row = wm * 64 + i * 16 + (lane & 15);
    aoff[i] = row * 64 + ((g ^ ((4 - ((row >> 2) & 3)) & 3)) << 4);
  }
  const int bo0 = (g * 12 + wn * 3) * 256 + ((q ^ (4 * (g & 1))) * 32) + p * 8;
  const int bo1 = (g * 12 + wn * 3) * 256 + (((4 + q) ^ (4 * (g & 1))) * 32) + p * 8;

  f32x4 acc[4][6];
#pragma unroll
  for (int i = 0; i < 4; ++i)
#pragma unroll
    for (int j = 0; j < 6; ++j) acc[i][j] = (f32x4){0.f, 0.f, 0.f, 0.f};

  bf16x8 af[2][4];
  s16x4 bl[2][6], bh[2][6];
  auto dsr = [&](int t, int set, int n) {
    const char* ab = As + (t & 3) * 8192;
    const char* bb = Bs + (t & 3) * 12288;
    if (n < 12) {
      const int j = n >> 1, o = (j < 3 ? j : j + 3) * 256;
      if (n & 1) bh[set][j] = __builtin_amdgcn_ds_read_tr16_b64_v4i16((lds_s16x4_ptr)(bb + bo1 + o));
      else bl[set][j] = __builtin_amdgcn_ds_read_tr16_b64_v4i16((lds_s16x4_ptr)(bb + bo0 + o));
    } else {
      af[set][n - 12] = *reinterpret_cast<const bf16x8*>(ab + aoff[n - 12]);
    }
  };
  auto gl = [&](int t, int gi) {
    if (t & 1) {
      if (gi < 4) { glA(t + 3 + (gi & 1), gi >> 1); return; }
      gi -= 4;
    }
    if (gi < 3) glB(t + 4, gi);
  };
  auto mm = [&](int set, int i) {
    const int mi = i / 6, j = i % 6;
    acc[mi][j] = __builtin_amdgcn_mfma_f32_16x16x32_bf16(__builtin_shufflevector(bl[set][j], bh[set][j], 0, 1, 2, 3, 4, 5, 6, 7),
                                                         af[set][mi], acc[mi][j], 0, 0, 0);
  };

  glA(0, 0); glA(1, 0); glA(0, 1); glA(1, 1);
  glB(0, 0); glB(0, 1); glB(0, 2); glB(1, 0); glB(1, 1); glB(1, 2);
  glA(2, 0); glA(3, 0); glA(2, 1); glA(3, 1);
  glB(2, 0); glB(2, 1); glB(2, 2); glB(3, 0); glB(3, 1); glB(3, 2);
  __builtin_amdgcn_s_setprio(1);
  asm volatile("s_waitcnt vmcnt(13)\n\ts_barrier" ::: "memory");
#pragma unroll
  for (int n = 0; n < 16; ++n) dsr(0, 0, n);
  SB();
#pragma unroll
  for (int t = 0; t < 16; ++t) {
    if (t < 15) {
      if (t == 14) asm volatile("s_waitcnt vmcnt(0) lgkmcnt(0)\n\ts_barrier" ::: "memory");
      else if (t == 13) asm volatile("s_waitcnt vmcnt(3) lgkmcnt(0)\n\ts_barrier" ::: "memory");
      else if (t & 1) asm volatile("s_waitcnt vmcnt(6) lgkmcnt(0)\n\ts_barrier" ::: "memory");
      else asm volatile("s_waitcnt vmcnt(10) lgkmcnt(0)\n\ts_barrier" ::: "memory");
    }
    SB();
#pragma unroll
    for (int i = 0; i < 24; ++i) {
      mm(t & 1, i);
      SB();
      if (t < 15 && i < 16) { dsr(t + 1, (t + 1) & 1, i); SB(); }
      if (t + 4 < 16 && (i % 3) == 2) {
        const int gi = i / 3;
        if (gi < ((t & 1) ? 7 : 3)) { gl(t, gi); SB(); }
      }
    }
  }
  asm volatile("s_waitcnt vmcnt(0) lgkmcnt(0)\n\ts_barrier" ::: "memory");

  constexpr int HS = 56;
  unsigned short* tl = reinterpret_cast<unsigned short*>(smem) + wid * (64 * HS);
#pragma unroll
  for (int hf = 0; hf < 2; ++hf) {
#pragma unroll
    for (int m2 = 0; m2 < 2; ++m2)
#pragma unroll
      for (int j = 0; j < 3; ++j) {
        const int mi = hf * 2 + m2;
        u32x2 o;
#pragma unroll
        for (int e = 0; e < 2; ++e) {
          const unsigned gp = pk_bf16(acc[mi][j][2 * e], acc[mi][j][2 * e + 1]);
          const unsigned up = pk_bf16(acc[mi][j + 3][2 * e], acc[mi][j + 3][2 * e + 1]);
          const float g0 = lo_f(gp), g1 = hi_f(gp);
          const unsigned sp = pk_bf16(g0 * sigm(g0), g1 * sigm(g1));
          o[e] = pk_bf16(lo_f(sp) * lo_f(up), hi_f(sp) * hi_f(up));
        }
        *reinterpret_cast<u32x2*>(tl + (mi * 16 + (lane & 15)) * HS + j * 16 + g * 4) = o;
      }
    wave_sync();
#pragma unroll
    for (int i = 0; i < 3; ++i) {
      const int c = lane + 64 * i, row = hf * 32 + c / 6, ch = c % 6;
      const int f = f0 + wn * 48 + ch * 8;
      if (f < FP_) {
        u32x4 v = *reinterpret_cast<const u32x4*>(tl + row * HS + ch * 8);
        if (f >= F_) v = (u32x4){0u, 0u, 0u, 0u};
        st16(H + (size_t)(m0 + wm * 64 + row) * FP_ + f, v);
      }
    }
    SB();
  }
}

DI void aux1_role(char* smem, int b, const unsigned short* __restrict__ x, const unsigned short* __restrict__ gw,
                  const unsigned short* __restrict__ wr, const unsigned short* __restrict__ wi, cf* __restrict__ XT,
                  float* __restrict__ G, unsigned* __restrict__ WT, const unsigned short* __restrict__ wd,
                  const unsigned short* __restrict__ wg, const unsigned short* __restrict__ wu) {
  const int tid = TID_X, j = tid & 31, hw = tid >> 5;
  unsigned pf = 0;
  if (tid < 192) {
    const int m = tid >> 6, l = tid & 63;
    if (l < 43) {
      const unsigned short* wsel = m == 0 ? wg : (m == 1 ? wu : wd);
      pf = reinterpret_cast<const unsigned*>(wsel)[((size_t)b * 43 + l) * 32];
    }
  }
  const int s0 = b * 8, s = s0 + hw;
  cf* sc = reinterpret_cast<cf*>(smem) + hw * RGN;
  const unsigned* xr = reinterpret_cast<const unsigned*>(x + (size_t)s * D_);
  const unsigned* gr = reinterpret_cast<const unsigned*>(gw);
  unsigned xv[8], gv[8];
#pragma unroll
  for (int r = 0; r < 8; ++r) xv[r] = xr[j + 32 * r];
#pragma unroll
  for (int r = 0; r < 8; ++r) gv[r] = gr[j + 32 * r];
  const u32x4* sr4 = reinterpret_cast<const u32x4*>(wr + (size_t)s0 * DF_);
  const u32x4* si4 = reinterpret_cast<const u32x4*>(wi + (size_t)s0 * DF_);
  const u32x4 w0 = sr4[tid], w1 = si4[tid];
  u32x4 w2 = w0;
  if (tid < 2) w2 = (tid == 0 ? sr4 : si4)[256];
  cf tw1[7], tw2[7];
  load_tw256(j, tw1, tw2);
  u32x4* lw = reinterpret_cast<u32x4*>(smem + 20480);
  lw[tid] = w0; lw[258 + tid] = w1;
  if (tid < 2) lw[tid * 258 + 256] = w2;

  cf v[8];
  float gs = 0.f;
#pragma unroll
  for (int r = 0; r < 8; ++r) {
    v[r] = mk(lo_f(xv[r]), hi_f(xv[r]));
    const unsigned pp = pk_bf16(v[r].x * lo_f(gv[r]), v[r].y * hi_f(gv[r]));
    gs += lo_f(pp);
    gs += hi_f(pp);
  }
#pragma unroll
  for (int o = 16; o > 0; o >>= 1) gs += __shfl_xor(gs, o);
  if (j == 0) G[s] = rbf(sigm(rbf(gs)));
  fft256_fwd(v, sc, j, tw1, tw2);
  {
    const int k1 = j >> 2, a = j & 3;
#pragma unroll
    for (int e = 0; e < 2; ++e)
#pragma unroll
      for (int l = 0; l < 4; ++l) sc[k1 + 8 * (a + 4 * e) + 64 * l] = v[e * 4 + l];
  }
  __syncthreads();
  {
    const int sp = (tid & 3) * 2, kk = tid >> 2;
    const cf* z0 = reinterpret_cast<const cf*>(smem) + sp * RGN;
    const cf* z1 = z0 + RGN;
#pragma unroll
    for (int i = 0; i < 2; ++i) {
      const int k = kk + 64 * i;
      if (k == 0) {
        const cf a0 = z0[0], ah = z0[128], b0 = z1[0], bh = z1[128];
        st16(XT + s0 + sp, f4u((f32x4){a0.x + a0.y, a0.x - a0.y, b0.x + b0.y, b0.x - b0.y}));
        st16(XT + (size_t)128 * XS_ + s0 + sp, f4u((f32x4){ah.x, -ah.y, bh.x, -bh.y}));
      } else {
        const cf w = tw_get(4 * k);
        f32x4 o0, o1;
#pragma unroll
        for (int rr = 0; rr < 2; ++rr) {
          const cf za = (rr ? z1 : z0)[k], zb = (rr ? z1 : z0)[256 - k];
          const cf E = mk(0.5f * (za.x + zb.x), 0.5f * (za.y - zb.y));
          const cf O = mk(0.5f * (za.y + zb.y), 0.5f * (zb.x - za.x));
          const cf wo = cmul(O, w);
          o0[2 * rr] = E.x + wo.x; o0[2 * rr + 1] = E.y + wo.y;
          o1[2 * rr] = E.x - wo.x; o1[2 * rr + 1] = wo.y - E.y;
        }
        st16(XT + (size_t)k * XS_ + s0 + sp, f4u(o0));
        st16(XT + (size_t)(256 - k) * XS_ + s0 + sp, f4u(o1));
      }
    }
  }
  {
    const unsigned short* fr = reinterpret_cast<const unsigned short*>(lw);
    const unsigned short* fi = reinterpret_cast<const unsigned short*>(lw + 258);
    const int r4 = (tid & 1) * 4;
#pragma unroll
    for (int it = 0; it < 3; ++it) {
      const int c = (tid >> 1) + 128 * it;
      if (c < DF_) {
        u32x4 o;
#pragma unroll
        for (int e = 0; e < 4; ++e) o[e] = (unsigned)fr[(r4 + e) * DF_ + c] | ((unsigned)fi[(r4 + e) * DF_ + c] << 16);
        st16(WT + (size_t)c * XS_ + s0 + r4, o);
      }
    }
  }
  asm volatile("" ::"v"(pf));
}

__global__ __launch_bounds__(256, 2) void k1_kernel(const unsigned short* __restrict__ x, const unsigned short* __restrict__ wr,
                                                    const unsigned short* __restrict__ wi, const unsigned short* __restrict__ wg,
                                                    const unsigned short* __restrict__ wu, const unsigned short* __restrict__ gw,
                                                    const unsigned short* __restrict__ wd, char* __restrict__ ws) {
  extern __shared__ __attribute__((aligned(16))) char smem[];
  const int b = BID_X;
  const int bg = b - AUX_WG, ba = b;
  if (b >= AUX_WG) {
    gemm1_role(smem, bg, x, wg, wu, reinterpret_cast<unsigned short*>(ws + WS_H));
  } else {
    aux1_role(smem, (ba & 7) * 32 + (ba >> 3), x, gw, wr, wi, reinterpret_cast<cf*>(ws + WS_XT), reinterpret_cast<float*>(ws + WS_G),
              reinterpret_cast<unsigned*>(ws + WS_WT), wd, wg, wu);
  }
}

constexpr int G2_TILES = 256;
constexpr int K2_GRID = G2_TILES + 256;
constexpr int K2_LDS = 81920;

DI void gemm2_role(char* smem, int b, const unsigned short* __restrict__ H, const unsigned short* __restrict__ wd,
                   unsigned short* __restrict__ Bo) {
  const int tid = TID_X, lane = tid & 63, wid = tid >> 6;
  const int xcd = b & 7, li = b >> 3;
  const int mt = xcd * 4 + (li >> 3), nt = li & 7;
  const int m0 = mt * 64, n0 = nt * 64;
  const unsigned short* asrc[2];
  int bk[2];
#pragma unroll
  for (int i = 0; i < 2; ++i) {
    const int row = (wid * 2 + i) * 8 + (lane >> 3), c = (lane & 7) ^ ((row >> 1) & 7);
    asrc[i] = H + (size_t)(m0 + row) * FP_ + c * 8;
    bk[i] = (wid * 2 + i) * 8 + (((lane >> 1) & 7) ^ (4 * ((lane >> 4) & 1)));
  }
  const unsigned short* bsrc = wd + n0 + (lane >> 4) * 16 + (lane & 1) * 8;
  auto gl = [&](int t, int i) {
    char* buf = smem + (t % 5) * 16384;
    if (i < 2) glds16(asrc[i] + t * 64, buf + (wid * 2 + i) * 1024);
    else {
      int k = t * 64 + bk[i - 2];
      if (t * 64 + 63 >= F_) k = k < F_ ? k : F_ - 1;
      glds16(bsrc + (size_t)k * D_, buf + 8192 + (wid * 2 + i - 2) * 1024);
    }
  };
  const int wm = wid >> 1, wn = wid & 1;
  const int h = lane >> 5, gi = lane >> 4, q = (lane & 15) >> 2, p = lane & 3;
  const int arow = wm * 32 + (lane & 31);
  int aoff[4];
#pragma unroll
  for (int ks = 0; ks < 4; ++ks) aoff[ks] = arow * 128 + (((2 * ks + h) ^ ((arow >> 1) & 7)) << 4);
  const int bbase = 8192 + (h * 4 + wn * 2 + (gi & 1)) * 256 + p * 8;
  const int bo0 = bbase + ((q ^ (4 * (gi & 1))) * 32), bo1 = bbase + (((4 + q) ^ (4 * (gi & 1))) * 32);
  f32x16 acc, acc2;
#pragma unroll
  for (int r = 0; r < 16; ++r) { acc[r] = 0.f; acc2[r] = 0.f; }
  bf16x8 a[2][4];
  s16x4 bl[2][4], bh[2][4];
  auto dsr = [&](int t, int set, int ks) {
    const char* buf = smem + (t % 5) * 16384;
    a[set][ks] = *reinterpret_cast<const bf16x8*>(buf + aoff[ks]);
    bl[set][ks] = __builtin_amdgcn_ds_read_tr16_b64_v4i16((lds_s16x4_ptr)(buf + bo0 + ks * 2048));
    bh[set][ks] = __builtin_amdgcn_ds_read_tr16_b64_v4i16((lds_s16x4_ptr)(buf + bo1 + ks * 2048));
  };
  constexpr int NT = 22;
#pragma unroll
  for (int t = 0; t < 5; ++t)
#pragma unroll
    for (int i = 0; i < 4; ++i) gl(t, i);
  __builtin_amdgcn_s_setprio(1);
  asm volatile("s_waitcnt vmcnt(16)\n\ts_barrier" ::: "memory");
#pragma unroll
  for (int i = 0; i < 4; ++i) dsr(0, 0, i);
  SB();
#pragma unroll
  for (int t = 0; t < NT; ++t) {
    if (t < NT - 1) {
      const int later = (t + 4 < NT ? t + 4 : NT - 1) - (t + 1);
      if (later == 3) asm volatile("s_waitcnt vmcnt(12) lgkmcnt(0)\n\ts_barrier" ::: "memory");
      else if (later == 2) asm volatile("s_waitcnt vmcnt(8) lgkmcnt(0)\n\ts_barrier" ::: "memory");
      else if (later == 1) asm volatile("s_waitcnt vmcnt(4) lgkmcnt(0)\n\ts_barrier" ::: "memory");
      else asm volatile("s_waitcnt vmcnt(0) lgkmcnt(0)\n\ts_barrier" ::: "memory");
    }
    SB();
#pragma unroll
    for (int i = 0; i < 4; ++i) {
      if (i & 1) acc2 = __builtin_amdgcn_mfma_f32_32x32x16_bf16(__builtin_shufflevector(bl[t & 1][i], bh[t & 1][i], 0, 1, 2, 3, 4, 5, 6, 7),
                                                                a[t & 1][i], acc2, 0, 0, 0);
      else acc = __builtin_amdgcn_mfma_f32_32x32x16_bf16(__builtin_shufflevector(bl[t & 1][i], bh[t & 1][i], 0, 1, 2, 3, 4, 5, 6, 7),
                                                         a[t & 1][i], acc, 0, 0, 0);
      SB();
      if (t < NT - 1) { dsr(t + 1, (t + 1) & 1, i); SB(); }
      if (t + 5 < NT) { gl(t + 5, i); SB(); }
    }
  }
#pragma unroll
  for (int r = 0; r < 16; ++r) acc[r] += acc2[r];
  unsigned short* drow = Bo + (size_t)(m0 + arow) * D_ + n0 + wn * 32 + h * 8;
#pragma unroll
  for (int rp = 0; rp < 2; ++rp) {
    unsigned e0 = pk_bf16(acc[8 * rp], acc[8 * rp + 1]), e1 = pk_bf16(acc[8 * rp + 2], acc[8 * rp + 3]);
    unsigned o0 = pk_bf16(acc[8 * rp + 4], acc[8 * rp + 5]), o1 = pk_bf16(acc[8 * rp + 6], acc[8 * rp + 7]);
    auto r0 = __builtin_amdgcn_permlane32_swap(e0, o0, false, false);
    auto r1 = __builtin_amdgcn_permlane32_swap(e1, o1, false, false);
    st16(drow + rp * 16, (u32x4){r0[0], r1[0], r0[1], r1[1]});
  }
}

DI int col_u(int t, int slot) {
  const int j = t & 31;
  return (t >> 5) + 8 * ((j >> 2) + 8 * ((j & 3) + 4 * (slot >> 2)) + 64 * (slot & 3));
}

DI void fft2_role(char* smem, int col, const cf* __restrict__ XT, const unsigned* __restrict__ WT, cf* __restrict__ YT) {
  const int t = TID_X, j = t & 31, hw = t >> 5;
  cf* all = reinterpret_cast<cf*>(smem);
  cf* sc = all + hw * RGN;
  const cf* xc = XT + (size_t)col * XS_;
  cf v[8];
#pragma unroll
  for (int r = 0; r < 8; ++r) v[r] = xc[t + 256 * r];
  unsigned w[8], wm[8], wn[8], wnm[8];
#pragma unroll
  for (int r = 0; r < 8; ++r) w[r] = WT[(size_t)col * XS_ + col_u(t, r)];
  if (col == 0) {
#pragma unroll
    for (int r = 0; r < 8; ++r) {
      const int u = col_u(t, r), um = (S_ - u) & (S_ - 1);
      wm[r] = WT[um]; wn[r] = WT[(size_t)256 * XS_ + u]; wnm[r] = WT[(size_t)256 * XS_ + um];
    }
  }
  cf tw0[7], tw1[7], tw2[7];
#pragma unroll
  for (int k = 1; k < 8; ++k) tw0[k - 1] = tw_get(t * k);
  load_tw256(j, tw1, tw2);

  dft8<-1>(v);
#pragma unroll
  for (int k = 1; k < 8; ++k) v[k] = cmul(v[k], tw0[k - 1]);
#pragma unroll
  for (int k = 0; k < 8; ++k) all[k * RGN + t] = v[k];
  __syncthreads();
#pragma unroll
  for (int r = 0; r < 8; ++r) v[r] = sc[j + 32 * r];
  wave_sync();
  fft256_fwd(v, sc, j, tw1, tw2);

  if (col != 0) {
#pragma unroll
    for (int r = 0; r < 8; ++r) v[r] = cmul(v[r], mk(lo_f(w[r]), hi_f(w[r])));
  } else {
    cf* nat = all + 8 * RGN;
#pragma unroll
    for (int r = 0; r < 8; ++r) { const int u = col_u(t, r); nat[(u & 7) * 260 + (u >> 3)] = v[r]; }
    __syncthreads();
#pragma unroll
    for (int r = 0; r < 8; ++r) {
      const int um = (S_ - col_u(t, r)) & (S_ - 1);
      const cf P = v[r], M = nat[(um & 7) * 260 + (um >> 3)];
      const cf X0 = mk(0.5f * (P.x + M.x), 0.5f * (P.y - M.y));
      const cf XN = mk(0.5f * (P.y + M.y), 0.5f * (M.x - P.x));
      const cf W0 = mk(0.5f * (lo_f(w[r]) + lo_f(wm[r])), 0.5f * (hi_f(w[r]) - hi_f(wm[r])));
      const cf WN = mk(0.5f * (lo_f(wn[r]) + lo_f(wnm[r])), 0.5f * (hi_f(wn[r]) - hi_f(wnm[r])));
      const cf Z0 = cmul(X0, W0), ZN = cmul(XN, WN);
      v[r] = mk(Z0.x - ZN.y, Z0.y + ZN.x);
    }
  }

  fft256_inv(v, sc, j, tw1, tw2);
#pragma unroll
  for (int r = 0; r < 8; ++r) sc[j + 32 * r] = v[r];
  __syncthreads();
#pragma unroll
  for (int k = 0; k < 8; ++k) v[k] = all[k * RGN + t];
#pragma unroll
  for (int k = 1; k < 8; ++k) v[k] = cmulc(v[k], tw0[k - 1]);
  dft8<1>(v);
#pragma unroll
  for (int r = 0; r < 8; ++r)
    st8(YT + ((size_t)((t >> 3) + 32 * r) * 256 + col) * 8 + (t & 7), (u32x2){__builtin_bit_cast(unsigned, v[r].x), __builtin_bit_cast(unsigned, v[r].y)});
}

__global__ __launch_bounds__(256, 2) void k2_kernel(const unsigned short* __restrict__ wd, char* __restrict__ ws) {
  extern __shared__ __attribute__((aligned(16))) char smem[];
  const int b = BID_X;
  if (b >= 256) {
    gemm2_role(smem, b - 256, reinterpret_cast<const unsigned short*>(ws + WS_H), wd, reinterpret_cast<unsigned short*>(ws + WS_B));
  } else {
    fft2_role(smem, (b & 7) * 32 + (b >> 3), reinterpret_cast<const cf*>(ws + WS_XT), reinterpret_cast<const unsigned*>(ws + WS_WT),
              reinterpret_cast<cf*>(ws + WS_YT));
  }
}

constexpr int R3 = 322;
__global__ __launch_bounds__(512) void k3_kernel(const char* __restrict__ ws, unsigned short* __restrict__ out) {
  __shared__ __attribute__((aligned(16))) cf smem[8 * R3];
  const int tid = TID_X, lane = tid & 63, wv = tid >> 6;
  const int blk = (BID_X & 7) * 32 + (BID_X >> 3);
  const int s0 = blk * 8, s = s0 + wv;
  const cf* YT = reinterpret_cast<const cf*>(ws + WS_YT) + (size_t)blk * 2048;
  const unsigned* Bo = reinterpret_cast<const unsigned*>(ws + WS_B) + (size_t)s * (D_ / 2);
  const float* G = reinterpret_cast<const float*>(ws + WS_G);
  const int sr = tid & 7, kk = tid >> 3;
  cf ya[2], yb[2], twk[2];
#pragma unroll
  for (int i = 0; i < 2; ++i) {
    const int k = kk + 64 * i;
    ya[i] = YT[k * 8 + sr];
    yb[i] = YT[(k == 0 ? 128 : 256 - k) * 8 + sr];
  }
  unsigned bv[4];
#pragma unroll
  for (int r = 0; r < 4; ++r) bv[r] = Bo[lane + 64 * r];
  const float g = G[s];
  cf t1[3], t2[3], t3[3];
#pragma unroll
  for (int i = 0; i < 2; ++i) twk[i] = tw_get(4 * (kk + 64 * i));
#pragma unroll
  for (int k = 1; k < 4; ++k) {
    t1[k - 1] = tw_get(8 * lane * k);
    t2[k - 1] = tw_get(32 * (lane & 15) * k);
    t3[k - 1] = tw_get(128 * (lane & 3) * k);
  }
  cf* zr = smem + sr * R3;
#pragma unroll
  for (int i = 0; i < 2; ++i) {
    const int k = kk + 64 * i;
    if (k == 0) {
      zr[0] = mk(ya[i].x + ya[i].y, ya[i].x - ya[i].y);
      zr[128] = mk(2.f * yb[i].x, -2.f * yb[i].y);
    } else {
      const cf A = mk(ya[i].x + yb[i].x, ya[i].y - yb[i].y);
      const cf Bv = mk(ya[i].x - yb[i].x, ya[i].y + yb[i].y);
      const cf wb = cmulc(Bv, twk[i]);
      const cf T = mk(-wb.y, wb.x);
      zr[k] = cadd(A, T);
      zr[256 - k] = mk(A.x - T.x, T.y - A.y);
    }
  }
  __syncthreads();
  cf* sc = smem + wv * R3;
  cf v[4];
  {
    const int kb = (lane >> 4) + 4 * ((lane >> 2) & 3) + 16 * (lane & 3);
#pragma unroll
    for (int l = 0; l < 4; ++l) v[l] = sc[kb + 64 * l];
  }
  wave_sync();
  dft4<1>(v[0], v[1], v[2], v[3]);
#pragma unroll
  for (int p2 = 0; p2 < 4; ++p2) sc[(lane & 3) * 65 + (lane & ~3) + p2] = v[p2];
  wave_sync();
#pragma unroll
  for (int i1 = 0; i1 < 4; ++i1) v[i1] = sc[i1 * 65 + lane];
  wave_sync();
#pragma unroll
  for (int k = 1; k < 4; ++k) v[k] = cmulc(v[k], t3[k - 1]);
  dft4<1>(v[0], v[1], v[2], v[3]);
#pragma unroll
  for (int p1 = 0; p1 < 4; ++p1) sc[(lane >> 2) * 20 + 4 * p1 + (lane & 3)] = v[p1];
  wave_sync();
#pragma unroll
  for (int j1 = 0; j1 < 4; ++j1) v[j1] = sc[((lane >> 4) * 4 + j1) * 20 + (lane & 15)];
  wave_sync();
#pragma unroll
  for (int k = 1; k < 4; ++k) v[k] = cmulc(v[k], t2[k - 1]);
  dft4<1>(v[0], v[1], v[2], v[3]);
#pragma unroll
  for (int m1 = 0; m1 < 4; ++m1) sc[(lane >> 4) * 80 + 16 * m1 + (lane & 15)] = v[m1];
  wave_sync();
#pragma unroll
  for (int k1 = 0; k1 < 4; ++k1) v[k1] = sc[k1 * 80 + lane];
#pragma unroll
  for (int k = 1; k < 4; ++k) v[k] = cmulc(v[k], t1[k - 1]);
  dft4<1>(v[0], v[1], v[2], v[3]);
  const float sc20 = 1.f / 1048576.f;
  const float og = rbf(1.f - g);
  unsigned* orow = reinterpret_cast<unsigned*>(out + (size_t)s * D_);
#pragma unroll
  for (int r = 0; r < 4; ++r) {
    const unsigned ap = pk_bf16(v[r].x * sc20, v[r].y * sc20);
    const unsigned ga = pk_bf16(g * lo_f(ap), g * hi_f(ap));
    const unsigned gb = pk_bf16(og * lo_f(bv[r]), og * hi_f(bv[r]));
    orow[lane + 64 * r] = pk_bf16(lo_f(ga) + lo_f(gb), hi_f(ga) + hi_f(gb));
  }
}

#ifdef POP_TARGET_ATTR
#pragma clang attribute pop
#endif

extern "C" void kernel_launch(void* const* d_in, const int* in_sizes, int n_in, void* d_out, int out_size, void* d_ws,
                              size_t ws_size, hipStream_t stream) {
  const unsigned short* x = (const unsigned short*)d_in[0];
  const unsigned short* wr = (const unsigned short*)d_in[1];
  const unsigned short* wi = (const unsigned short*)d_in[2];
  const unsigned short* wg = (const unsigned short*)d_in[4];
  const unsigned short* wu = (const unsigned short*)d_in[5];
  const unsigned short* wd = (const unsigned short*)d_in[6];
  const unsigned short* gw = (const unsigned short*)d_in[7];
  char* ws = (char*)d_ws;
  (void)hipFuncSetAttribute((const void*)k1_kernel, hipFuncAttributeMaxDynamicSharedMemorySize, K1_LDS);
  (void)hipFuncSetAttribute((const void*)k2_kernel, hipFuncAttributeMaxDynamicSharedMemorySize, K2_LDS);
  hipLaunchKernelGGL(k1_kernel, dim3(K1_GRID), dim3(256), K1_LDS, stream, x, wr, wi, wg, wu, gw, wd, ws);
  hipLaunchKernelGGL(k2_kernel, dim3(K2_GRID), dim3(256), K2_LDS, stream, wd, ws);
  hipLaunchKernelGGL(k3_kernel, dim3(S_ / 8), dim3(512), 0, stream, (const char*)ws, (unsigned short*)d_out);
}
```
